```python
import torch

def _softmax(x, axis=-1):
    return torch.softmax(x, dim=axis)
def _gelu(x):
    return torch.nn.functional.gelu(x, approximate="tanh")
def _silu(x):
    return torch.nn.functional.silu(x)
def _sigmoid(x):
    return torch.sigmoid(x)
def _layernorm(x, axis=-1, eps=1e-5):
    m = x.mean(dim=axis, keepdim=True)
    v = x.var(dim=axis, keepdim=True, unbiased=False)
    return (x - m) / torch.sqrt(v + eps)
def _scatter_add(tgt, idx, src, axis=0):
    return tgt.clone().index_add_(axis, idx, src)
def _segment_sum(src, seg, n):
    out = torch.zeros((n,) + src.shape[1:], dtype=src.dtype)
    return out.index_add_(0, seg, src)
def _pad(x, pw):
    flat = tuple(v for pair in reversed(pw) for v in pair)
    return torch.nn.functional.pad(x, flat)

kernel_name = 'recurrent_scan__sliding_block_cached__swiglu_ffn__decode'

def setup_inputs():

    _w = (lambda a: (lambda t: t.to(torch.bfloat16) if t.is_floating_point() else t)(torch.as_tensor(a)))
    def _A_setup():
        B, Sq, D = 1, 1, 448
        Hq, Hkv, Sk, Dh = 16, 8, 65536, 256
        assert Hq % Hkv == 0, (Hq, Hkv)
        return {
            "x": _w((lambda *s: torch.randn(*s, dtype=torch.bfloat16) * 0.05)(B, Sq, D)),
            "k_cache": _w((lambda *s: torch.randn(*s, dtype=torch.bfloat16) * 1.6)(B, Hkv, Sk, Dh)),
            "v_cache": _w((lambda *s: torch.randn(*s, dtype=torch.bfloat16) * 0.2)(B, Hkv, Sk, Dh)),
            "wq": _w((lambda *s: torch.randn(*s, dtype=torch.bfloat16) * 1.6)(D, Hq * Dh) * (D ** -0.5)),
            "wo": _w((lambda *s: torch.randn(*s, dtype=torch.bfloat16) * 0.2)(Hq * Dh, D) * ((Hq * Dh) ** -0.5)),
        }
    _A_in = _A_setup()

    def _B_setup():
        B, S, D, F = 1, 1, 448, 1792
        return {
            "x": _w((lambda *s: torch.randn(*s, dtype=torch.bfloat16) * 0.05)(B, S, D)),
            "w_gate": _w((lambda *s: torch.randn(*s, dtype=torch.bfloat16) * 0.1)(D, F)),
            "w_up": _w((lambda *s: torch.randn(*s, dtype=torch.bfloat16) * 0.1)(D, F)),
            "w_down": _w((lambda *s: torch.randn(*s, dtype=torch.bfloat16) * 0.1)(F, D)),
        }
    _B_in = _B_setup()

    return {
        "A_x": _A_in["x"],
        "A_k_cache": _A_in["k_cache"],
        "A_v_cache": _A_in["v_cache"],
        "A_wq": _A_in["wq"],
        "A_wo": _A_in["wo"],
        "B_x": _B_in["x"],
        "B_w_gate": _B_in["w_gate"],
        "B_w_up": _B_in["w_up"],
        "B_w_down": _B_in["w_down"],
    }

def reference(A_x, A_k_cache, A_v_cache, A_wq, A_wo, B_x, B_w_gate, B_w_up, B_w_down):
    def _A_ref(x, k_cache, v_cache, wq, wo):
        B, Sq, D = x.shape
        Hq, Dh = 16, 256
        Hkv, Sk = k_cache.shape[1], k_cache.shape[2]
        W = min(4096, max(256, Sk // 4))
        rep = Hq // Hkv
        q = (lambda x, *d: x.permute(*d))((x @ wq).reshape(B, Sq, Hq, Dh), 0, 2, 1, 3)
        k = (lambda x, n, axis: torch.repeat_interleave(x, n, dim=axis))(k_cache[:, :, Sk - W:, :], rep, axis=1)
        v = (lambda x, n, axis: torch.repeat_interleave(x, n, dim=axis))(v_cache[:, :, Sk - W:, :], rep, axis=1)
        scores = torch.einsum("bhqd,bhkd->bhqk", q, k) * (Dh ** -0.5)
        p = _softmax(scores, axis=-1)
        o = torch.einsum("bhqk,bhkd->bhqd", p, v)
        return (lambda x, *d: x.permute(*d))(o, 0, 2, 1, 3).reshape(B, Sq, Hq * Dh) @ wo

    def _B_ref(x, w_gate, w_up, w_down):
        g = _silu(x @ w_gate)
        u = x @ w_up
        return (g * u) @ w_down

    x = A_x
    xn = _layernorm(x, axis=-1)
    s = _A_ref(xn, A_k_cache, A_v_cache, A_wq, A_wo)
    g = _silu(_B_ref(xn, B_w_gate, B_w_up, B_w_down))
    return x + s * g
```

```cpp
#include <hip/hip_runtime.h>
#include <hip/hip_bf16.h>
#include <stdint.h>

typedef unsigned short u16;
typedef unsigned int u32;
typedef u32 u32x4 __attribute__((ext_vector_type(4)));

constexpr int D = 448, HQ = 16, HKV = 8, SK = 65536, DH = 256, W = 4096, F = 1792;

__device__ __forceinline__ float bf2f(u16 v) { return __uint_as_float(((u32)v) << 16); }
__device__ __forceinline__ float rbf(float f) { return __bfloat162float(__float2bfloat16(f)); }
__device__ __forceinline__ float lo(u32 v) { return __uint_as_float(v << 16); }
__device__ __forceinline__ float hi(u32 v) { return __uint_as_float(v & 0xffff0000u); }

__device__ void layernorm(const u16* x, float* xn  , float* red) {
    int t = threadIdx.x;
    if (t < 64) {
        float v[7]; float s = 0.f;
        for (int i = 0; i < 7; ++i) { v[i] = bf2f(x[t + 64 * i]); s += v[i]; }
        for (int o = 32; o; o >>= 1) s += __shfl_xor(s, o);
        float mean = s * (1.f / D);
        float q = 0.f;
        for (int i = 0; i < 7; ++i) { float d = v[i] - mean; q += d * d; }
        for (int o = 32; o; o >>= 1) q += __shfl_xor(q, o);
        float var = rbf(q * (1.f / D));
        float mb = rbf(mean);
        float den = rbf(sqrtf(rbf(var + 1e-5f)));
        for (int i = 0; i < 7; ++i) xn[t + 64 * i] = rbf(rbf(v[i] - mb) / den);
    }
    __syncthreads();
}

__global__ void __launch_bounds__(256) k1(const u16* x, const u16* wq, const u16* wg, const u16* wu, float* q, float* h) {
    __shared__ float xn[D]; __shared__ float red[2][4][64];
    layernorm(x, xn, nullptr);
    int b = blockIdx.x, t = threadIdx.x, c = t & 63, kg = t >> 6;
    if (b < 64) {
        int col = b * 64 + c; float a = 0.f;
        for (int k = kg * 112; k < kg * 112 + 112; ++k) a += xn[k] * bf2f(wq[(size_t)k * 4096 + col]);
        red[0][kg][c] = a; __syncthreads();
        if (kg == 0) q[col] = rbf(red[0][0][c] + red[0][1][c] + red[0][2][c] + red[0][3][c]);
    } else {
        int col = (b - 64) * 64 + c; float a = 0.f, u = 0.f;
        for (int k = kg * 112; k < kg * 112 + 112; ++k) { a += xn[k] * bf2f(wg[(size_t)k * F + col]); u += xn[k] * bf2f(wu[(size_t)k * F + col]); }
        red[0][kg][c] = a; red[1][kg][c] = u; __syncthreads();
        if (kg == 0) {
            float g = rbf(red[0][0][c] + red[0][1][c] + red[0][2][c] + red[0][3][c]);
            float uu = rbf(red[1][0][c] + red[1][1][c] + red[1][2][c] + red[1][3][c]);
            float sg = rbf(g / (1.f + __expf(-g)));
            h[col] = rbf(sg * uu);
        }
    }
}

__global__ void __launch_bounds__(256) k2(const u16* kc, const u16* vc, const float* q, float* po, float* pml) {
    __shared__ float qs[512]; __shared__ float sc[2][128]; __shared__ float acc[4][512]; __shared__ float ml[4];
    int b = blockIdx.x, h = b & 7, j = b >> 3, t = threadIdx.x, lane = t & 63, w = t >> 6;
    qs[t] = q[h * 512 + t]; qs[t + 256] = q[h * 512 + 256 + t];
    __syncthreads();
    const u16* kb = kc + ((size_t)h * SK + (SK - W) + j * 128) * DH;
    const u16* vb = vc + ((size_t)h * SK + (SK - W) + j * 128) * DH;
    int half = lane >> 5, c = lane & 31;
    for (int i = 0; i < 16; ++i) {
        int key = w * 32 + i * 2 + half;
        u32x4 kv = *(const u32x4*)(kb + (size_t)key * DH + c * 8);
        float s0 = 0.f, s1 = 0.f;
        for (int e = 0; e < 4; ++e) {
            s0 += lo(kv[e]) * qs[c * 8 + 2 * e] + hi(kv[e]) * qs[c * 8 + 2 * e + 1];
            s1 += lo(kv[e]) * qs[256 + c * 8 + 2 * e] + hi(kv[e]) * qs[256 + c * 8 + 2 * e + 1];
        }
        for (int o = 16; o; o >>= 1) { s0 += __shfl_xor(s0, o); s1 += __shfl_xor(s1, o); }
        if (c == 0) { sc[0][key] = rbf(s0) * 0.0625f; sc[1][key] = rbf(s1) * 0.0625f; }
    }
    __syncthreads();
    if (w < 2) {
        float a = sc[w][lane], bb = sc[w][lane + 64];
        float m = fmaxf(a, bb);
        for (int o = 32; o; o >>= 1) m = fmaxf(m, __shfl_xor(m, o));
        a = __expf(a - m); bb = __expf(bb - m);
        float l = a + bb;
        for (int o = 32; o; o >>= 1) l += __shfl_xor(l, o);
        sc[w][lane] = a; sc[w][lane + 64] = bb;
        if (lane == 0) { ml[w * 2] = m; ml[w * 2 + 1] = l; }
    }
    __syncthreads();
    float a0[8] = {0}, a1[8] = {0};
    for (int i = 0; i < 16; ++i) {
        int key = w * 32 + i * 2 + half;
        u32x4 vv = *(const u32x4*)(vb + (size_t)key * DH + c * 8);
        float p0 = sc[0][key], p1 = sc[1][key];
        for (int e = 0; e < 4; ++e) {
            a0[2 * e] += p0 * lo(vv[e]); a0[2 * e + 1] += p0 * hi(vv[e]);
            a1[2 * e] += p1 * lo(vv[e]); a1[2 * e + 1] += p1 * hi(vv[e]);
        }
    }
    for (int e = 0; e < 8; ++e) { a0[e] += __shfl_xor(a0[e], 32); a1[e] += __shfl_xor(a1[e], 32); }
    if (half == 0) for (int e = 0; e < 8; ++e) { acc[w][c * 8 + e] = a0[e]; acc[w][256 + c * 8 + e] = a1[e]; }
    __syncthreads();
    for (int i = t; i < 512; i += 256) po[(size_t)(h * 32 + j) * 512 + i] = acc[0][i] + acc[1][i] + acc[2][i] + acc[3][i];
    if (t < 4) pml[(h * 32 + j) * 4 + t] = ml[t];
}

__global__ void __launch_bounds__(1024) k3(const u16* x, const u16* wo, const u16* wd, const float* po, const float* pml, const float* hh, u16* out) {
    __shared__ float o[4096]; __shared__ float hs[F]; __shared__ float red[2][16][64];
    int t = threadIdx.x;
    for (int i = t; i < 4096; i += 1024) {
        int h = i >> 9, hd = (i >> 8) & 1, r = i & 511;
        float M = -INFINITY;
        _Pragma("unroll 4") for (int j = 0; j < 32; ++j) M = fmaxf(M, pml[(h * 32 + j) * 4 + hd * 2]);
        float L = 0.f, a = 0.f;
        _Pragma("unroll 4") for (int j = 0; j < 32; ++j) { float wgt = __expf(pml[(h * 32 + j) * 4 + hd * 2] - M); L += wgt * pml[(h * 32 + j) * 4 + hd * 2 + 1]; a += wgt * po[(size_t)(h * 32 + j) * 512 + r]; }
        o[i] = rbf(a / L);
    }
    for (int i = t; i < F; i += 1024) hs[i] = hh[i];
    __syncthreads();
    int c = t & 63, kg = t >> 6, col = blockIdx.x * 64 + c;
    float a = 0.f, f = 0.f;
    for (int k = kg * 256; k < kg * 256 + 256; ++k) a += o[k] * bf2f(wo[(size_t)k * D + col]);
    for (int k = kg * 112; k < kg * 112 + 112; ++k) f += hs[k] * bf2f(wd[(size_t)k * D + col]);
    red[0][kg][c] = a; red[1][kg][c] = f; __syncthreads();
    if (kg == 0) {
        float s = 0.f, ff = 0.f;
        for (int i = 0; i < 16; ++i) { s += red[0][i][c]; ff += red[1][i][c]; }
        s = rbf(s); ff = rbf(ff);
        float g = rbf(ff / (1.f + __expf(-ff)));
        float r = rbf(bf2f(x[col]) + rbf(s * g));
        out[col] = (u16)(__float_as_uint(r) >> 16);
    }
}

extern "C" void kernel_launch(void* const* d_in, const int* in_sizes, int n_in, void* d_out, int out_size, void* d_ws, size_t ws_size, hipStream_t stream) {
    const u16* x = (const u16*)d_in[0]; const u16* kc = (const u16*)d_in[1]; const u16* vc = (const u16*)d_in[2];
    const u16* wq = (const u16*)d_in[3]; const u16* wo = (const u16*)d_in[4];
    const u16* wg = (const u16*)d_in[6]; const u16* wu = (const u16*)d_in[7]; const u16* wd = (const u16*)d_in[8];
    float* ws = (float*)d_ws;
    float* q = ws; float* h = ws + 4096; float* pml = ws + 8192; float* po = ws + 16384;
    k1<<<64 + 28, 256, 0, stream>>>(x, wq, wg, wu, q, h);
    k2<<<256, 256, 0, stream>>>(kc, vc, q, po, pml);
    k3<<<7, 1024, 0, stream>>>(x, wo, wd, po, pml, h, (u16*)d_out);
}
```

```cpp
#include <hip/hip_runtime.h>
#include <hip/hip_bf16.h>
#include <stdint.h>

typedef unsigned short u16;
typedef unsigned int u32;
typedef unsigned long long u64;
typedef u32 u32x4 __attribute__((ext_vector_type(4)));
typedef u32 u32x2 __attribute__((ext_vector_type(2)));
typedef float f32x2 __attribute__((ext_vector_type(2)));
typedef float f32x4 __attribute__((ext_vector_type(4)));
typedef __bf16 bf16x2 __attribute__((ext_vector_type(2)));
typedef __amdgpu_buffer_rsrc_t rsrc_t;

constexpr int D = 448, SK = 65536, DH = 256, WIN_KEYS = 4096, F = 1792;
constexpr u32 WS_EPOCH = 0;
constexpr u32 WS_QP = 4096;
constexpr u32 WS_GU = WS_QP + 8 * 4 * 512 * 8;
constexpr u32 WS_ST = WS_GU + 32 * 8 * 8 * 128;
constexpr u32 WS_OP = WS_ST + 8 * 32 * 32;
constexpr u32 WS_SP = WS_OP + 8 * 32 * 512 * 8;
constexpr u32 WS_END = WS_SP + 256 * 448 * 16;
constexpr int AUX_NT = 2, AUX_SC1 = 16, AUX_POLL = 16;
constexpr u32 SPIN_CAP = 1u << 17;

#define SB __builtin_amdgcn_sched_barrier(0)
#define PIN(x) do { asm volatile("" :: "v"(x)); SB; } while (0)

__device__ __forceinline__ rsrc_t mk(const void* p, u32 bytes) { return __builtin_amdgcn_make_buffer_rsrc((void*)p, 0, bytes, 0x00020000); }
template <int AUX> __device__ __forceinline__ u32x4 ld16(rsrc_t r, u32 voff, u32 soff) { return __builtin_amdgcn_raw_buffer_load_b128(r, voff, soff, AUX); }
__device__ __forceinline__ void st16(rsrc_t r, u32x4 v, u32 voff, u32 soff) { __builtin_amdgcn_raw_buffer_store_b128(v, r, voff, soff, AUX_SC1); }
__device__ __forceinline__ void st8(rsrc_t r, u32x2 v, u32 voff, u32 soff) { __builtin_amdgcn_raw_buffer_store_b64(v, r, voff, soff, AUX_SC1); }
__device__ __forceinline__ float asf(u32 v) { return __builtin_bit_cast(float, v); }
__device__ __forceinline__ u32 asu(float v) { return __builtin_bit_cast(u32, v); }
__device__ __forceinline__ float blo(u32 v) { return asf(v << 16); }
__device__ __forceinline__ float bhi(u32 v) { return asf(v & 0xffff0000u); }
__device__ __forceinline__ float rbf(float f) { return (float)(__bf16)f; }
__device__ __forceinline__ float dot2(u32 a, u32 b, float c) { return __builtin_amdgcn_fdot2_f32_bf16(__builtin_bit_cast(bf16x2, a), __builtin_bit_cast(bf16x2, b), c, false); }
template <int CTRL> __device__ __forceinline__ float dpp(float x) { return asf(__builtin_amdgcn_mov_dpp((int)asu(x), CTRL, 0xf, 0xf, true)); }
constexpr int XOR1 = 0xB1, XOR2 = 0x4E, XOR7 = 0x141, ROR8 = 0x128, ROR4 = 0x124;
__device__ __forceinline__ float tr32(float x, float y) { auto s = __builtin_amdgcn_permlane32_swap(asu(x), asu(y), false, false); return asf(s[0]) + asf(s[1]); }
__device__ __forceinline__ float tr16(float x, float y) { auto s = __builtin_amdgcn_permlane16_swap(asu(x), asu(y), false, false); return asf(s[0]) + asf(s[1]); }
template <int CTRL> __device__ __forceinline__ float trd(float x, float y, bool sel) { float a = x + dpp<CTRL>(x), b = y + dpp<CTRL>(y); return sel ? b : a; }
__device__ __forceinline__ float max32(float x) { auto s = __builtin_amdgcn_permlane32_swap(asu(x), asu(x), false, false); return fmaxf(asf(s[0]), asf(s[1])); }
__device__ __forceinline__ float max16(float x) { auto s = __builtin_amdgcn_permlane16_swap(asu(x), asu(x), false, false); return fmaxf(asf(s[0]), asf(s[1])); }
__device__ __forceinline__ float sum_hi3(float x) { x += dpp<ROR8>(x); x = tr16(x, x); return tr32(x, x); }
__device__ __forceinline__ float sum_hi5(float x) { x += dpp<XOR2>(x); x += dpp<ROR4>(x); return sum_hi3(x); }
__device__ __forceinline__ float sum_all(float x) { x += dpp<XOR1>(x); return sum_hi5(x); }
__device__ __forceinline__ float max_hi5(float x) { x = fmaxf(x, dpp<XOR2>(x)); x = fmaxf(x, dpp<ROR4>(x)); x = fmaxf(x, dpp<ROR8>(x)); return max32(max16(x)); }
__device__ __forceinline__ float max_all(float x) { x = fmaxf(x, dpp<XOR1>(x)); return max_hi5(x); }
__device__ __forceinline__ float silu(float v) { return v / (1.f + __expf(-v)); }
__device__ __forceinline__ void bar() { asm volatile("s_waitcnt lgkmcnt(0)\n\ts_barrier" ::: "memory"); }
__device__ __forceinline__ u32 bad(u32x4 g, u32 tag) { return (g[1] ^ tag) | (g[3] ^ tag); }
__device__ __forceinline__ bool none(u32 v) { return __builtin_amdgcn_ballot_w64(v != 0) == 0; }

#define POLL(COND, RELOAD, SLEEP) u32 spins = 0; if (__builtin_expect(!(COND), 0)) { do { if (SLEEP) __builtin_amdgcn_s_sleep(1); asm volatile("" ::: "memory"); RELOAD; } while (!(COND) && ++spins < SPIN_CAP); }
#define STAMP(i)

__global__ void __launch_bounds__(256) fused_kernel(const u16* __restrict__ x, const u16* __restrict__ kc, const u16* __restrict__ vc,
                                                    const u16* __restrict__ wq, const u16* __restrict__ wo, const u16* __restrict__ wg,
                                                    const u16* __restrict__ wu, const u16* __restrict__ wd, u16* __restrict__ out, char* ws) {
    __shared__ __attribute__((aligned(16))) float lds[4 * 448 * 2 + 64 + 256 + 256 + 4 * 256 + 64];
    float* l_xn = lds;
    float* l_q1 = lds + 4 * 448 * 2;
    u32* l_q = (u32*)(l_q1 + 64);
    float* l_sc = l_q1 + 64 + 256;
    float* l_p = l_sc + 256;
    float* l_fin = l_p + 4 * 256;
    const u32 b = blockIdx.x, h = b & 7, j = b >> 3;
    const u32 t = threadIdx.x, l = t & 63, w = __builtin_amdgcn_readfirstlane(t >> 6);
    const rsrc_t rws = mk(ws, WS_END);
    const size_t kvoff = ((size_t)h * SK + (SK - WIN_KEYS) + j * 128) * DH;
    const rsrc_t rk = mk(kc + kvoff, 65536), rv = mk(vc + kvoff, 65536);
    const u32 cgf = h + 8 * (j >> 3), rgf = j & 7;
    const bool ffn = cgf < 28;
    const u32 cg = j & 7, rg = j >> 3;
    const u16* wbase = w < 2 ? wq : (w == 2 ? wg : wu);
    const u32 wbytes = w < 2 ? D * 4096 * 2 : (ffn ? D * F * 2 : 0);
    const u32 wstride = w < 2 ? 8192 : F * 2;
    const u32 row0 = w < 2 ? rg * 112 + w * 56 : rgf * 56;
    const u32 colb = w < 2 ? (h * 512 + cg * 64) * 2 : cgf * 128;
    const rsrc_t rw = mk(wbase, wbytes);

    const u32 ep = __builtin_amdgcn_raw_buffer_load_b32(rws, 0, WS_EPOCH, AUX_SC1);
    const u32x4 xr = ld16<0>(mk(x, D * 2), l * 16, 0);
    SB;
    u32x4 W[7], K[16], V[16], WD[2], WO[4];
    {
        const u32 voff = (row0 + (l >> 3)) * wstride + colb + (l & 7) * 16;
#pragma unroll
        for (int i = 0; i < 7; ++i) { W[i] = ld16<AUX_NT>(rw, voff, i * 8 * wstride); SB; }
    }
    const u32 kvo = l * 16 + w * 1024;
    const u32 vvo = (l >> 3) * 512 + w * 128 + (l & 7) * 16;
#define LDK(i) do { K[i] = ld16<AUX_NT>(rk, kvo, (i) * 4096); SB; } while (0)
#define LDV(i) do { V[i] = ld16<AUX_NT>(rv, vvo, (i) * 4096); SB; } while (0)
    LDK(0); LDK(1); LDK(2); LDK(3);
    STAMP(1);
    float xv[8];
#pragma unroll
    for (int e = 0; e < 4; ++e) { xv[2 * e] = blo(xr[e]); xv[2 * e + 1] = bhi(xr[e]); }
    {
        float s = 0.f;
#pragma unroll
        for (int e = 0; e < 8; ++e) s += xv[e];
        const float mean = sum_all(s) * (1.f / D);
        float q = 0.f;
#pragma unroll
        for (int e = 0; e < 8; ++e) { const float d = xv[e] - mean; q += d * d; }
        q = l < 56 ? q : 0.f;
        const float var = rbf(sum_all(q) * (1.f / D));
        const float mb = rbf(mean);
        const float den = rbf(__builtin_sqrtf(rbf(var + 1e-5f)));
        if (l < 56) {
            f32x4 a, c;
#pragma unroll
            for (int e = 0; e < 4; ++e) { a[e] = rbf(rbf(xv[e] - mb) / den); c[e] = rbf(rbf(xv[e + 4] - mb) / den); }
            *(f32x4*)(l_xn + w * 448 + l * 8) = a; *(f32x4*)(l_xn + w * 448 + l * 8 + 4) = c;
        }
    }
    const u32 tag = __builtin_amdgcn_readfirstlane(ep) + 1u;
    float xo;
    {
        const u32 x0 = __builtin_amdgcn_readlane(xr[0], b & 63), x1 = __builtin_amdgcn_readlane(xr[1], b & 63), x2 = __builtin_amdgcn_readlane(xr[2], b & 63), x3 = __builtin_amdgcn_readlane(xr[3], b & 63);
        const u32 xw = (l & 4) ? ((l & 2) ? x3 : x2) : ((l & 2) ? x1 : x0);
        xo = (l & 1) ? bhi(xw) : blo(xw);
    }
    STAMP(2);
    {
        float xs[7];
#pragma unroll
        for (int i = 0; i < 7; ++i) xs[i] = l_xn[w * 448 + row0 + 8 * i + (l >> 3)];
        float a[8];
#pragma unroll
        for (int e = 0; e < 8; ++e) a[e] = 0.f;
#pragma unroll
        for (int i = 0; i < 7; ++i) {
#pragma unroll
            for (int e = 0; e < 4; ++e) { a[2 * e] = fmaf(xs[i], blo(W[i][e]), a[2 * e]); a[2 * e + 1] = fmaf(xs[i], bhi(W[i][e]), a[2 * e + 1]); }
            SB;
            LDK(4 + i);
        }
#pragma unroll
        for (int e = 0; e < 4; ++e) a[e] = tr32(a[e], a[e + 4]);
        a[0] = tr16(a[0], a[2]); a[1] = tr16(a[1], a[3]);
        float r = trd<ROR8>(a[0], a[1], (l & 8) != 0);
        const u32 col = (l & 7) * 8 + (l >> 3);
        if (w == 1) l_q1[l] = r;
        if (w >= 2 && ffn) st8(rws, u32x2{asu(r), tag}, ((l & 7) * 8 + rgf) * 128 + ((w - 2) * 8 + (l >> 3)) * 8, WS_GU + cgf * 8192);
        bar();
        if (w == 0) { r += l_q1[l]; st8(rws, u32x2{asu(r), tag}, (cg * 64 + col) * 8, WS_QP + (h * 4 + rg) * 4096); }
        SB;
    }
    STAMP(3);
    PIN(K[0]); LDK(11); PIN(K[1]); LDK(12); PIN(K[2]); LDK(13); PIN(K[3]); LDK(14); PIN(K[4]); LDK(15);
    PIN(K[5]);
    u32x4 qg[4], gu;
    const u32 guo = WS_GU + cgf * 8192 + rgf * 1024;
#pragma unroll
    for (int i = 0; i < 4; ++i) qg[i] = ld16<AUX_POLL>(rws, t * 16, WS_QP + (h * 4 + i) * 4096);
    gu = ld16<AUX_POLL>(rws, l * 16, guo);
    SB;
    STAMP(4);
    PIN(K[6]); LDV(0); PIN(K[7]); LDV(1); PIN(K[8]); LDV(2); PIN(K[9]); LDV(3); PIN(K[10]); LDV(4);
    PIN(K[11]); LDV(5); PIN(K[12]); LDV(6); PIN(K[13]); LDV(7); PIN(K[14]); LDV(8); PIN(K[15]); LDV(9);
    STAMP(5);
    {
        POLL(none(bad(qg[0], tag) | bad(qg[1], tag) | bad(qg[2], tag) | bad(qg[3], tag)),
             _Pragma("unroll") for (int i = 0; i < 4; ++i) qg[i] = ld16<AUX_POLL>(rws, t * 16, WS_QP + (h * 4 + i) * 4096), 1)
        const float q0 = asf(qg[0][0]) + asf(qg[1][0]) + asf(qg[2][0]) + asf(qg[3][0]);
        const float q1 = asf(qg[0][2]) + asf(qg[1][2]) + asf(qg[2][2]) + asf(qg[3][2]);
        l_q[t] = __builtin_bit_cast(u32, bf16x2{(__bf16)q0, (__bf16)q1});
    }
    bar();
    STAMP(6);
    {
        const u32x4 q0 = *(const u32x4*)(l_q + (l & 31) * 4), q1 = *(const u32x4*)(l_q + 128 + (l & 31) * 4);
        float s[32];
#pragma unroll
        for (int i = 0; i < 16; ++i) {
            float a = 0.f, c = 0.f;
#pragma unroll
            for (int e = 0; e < 4; ++e) { a = dot2(K[i][e], q0[e], a); c = dot2(K[i][e], q1[e], c); }
            s[2 * i] = a; s[2 * i + 1] = c;
        }
#pragma unroll
        for (int k = 0; k < 16; ++k) s[k] = tr16(s[k], s[k + 16]);
#pragma unroll
        for (int k = 0; k < 8; ++k) s[k] = trd<ROR8>(s[k], s[k + 8], (l & 8) != 0);
#pragma unroll
        for (int k = 0; k < 4; ++k) s[k] = trd<XOR7>(s[k], s[k + 4], (l & 4) != 0);
#pragma unroll
        for (int k = 0; k < 2; ++k) s[k] = trd<XOR2>(s[k], s[k + 2], (l & 2) != 0);
        const float sc = trd<XOR1>(s[0], s[1], (l & 1) != 0);
        const u32 key = 8 * ((l & 31) >> 1) + 2 * w + (l >> 5);
        l_sc[key * 2 + (l & 1)] = rbf(sc) * (0.0625f * 1.4426950408889634f);
    }
    bar();
    f32x2 P[16];
    {
        const f32x4 s4 = *(const f32x4*)(l_sc + l * 4);
        const float m0 = max_all(fmaxf(s4[0], s4[2])), m1 = max_all(fmaxf(s4[1], s4[3]));
        f32x4 p4;
        p4[0] = __builtin_amdgcn_exp2f(s4[0] - m0); p4[1] = __builtin_amdgcn_exp2f(s4[1] - m1);
        p4[2] = __builtin_amdgcn_exp2f(s4[2] - m0); p4[3] = __builtin_amdgcn_exp2f(s4[3] - m1);
        *(f32x4*)(l_p + w * 256 + l * 4) = p4;
        if (w == 0) {
            const float l0 = sum_all(p4[0] + p4[2]), l1 = sum_all(p4[1] + p4[3]);
            if (l < 2) st16(rws, u32x4{asu(l ? m1 : m0), tag, asu(l ? l1 : l0), tag}, l * 16, WS_ST + (h * 32 + j) * 32);
        }
#pragma unroll
        for (int i = 0; i < 16; ++i) P[i] = *(const f32x2*)(l_p + w * 256 + (8 * i + (l >> 3)) * 2);
    }
    STAMP(7);
    const u32 rowo = l < 56 ? l * 16 : 0x80000000u;
    const rsrc_t rwo = mk(wo, 4096 * D * 2), rwd = mk(wd, ffn ? F * D * 2 : 0);
    u32x4 stt;
    {
        f32x2 a0[4], a1[4];
#pragma unroll
        for (int e = 0; e < 4; ++e) { a0[e] = f32x2{0.f, 0.f}; a1[e] = f32x2{0.f, 0.f}; }
#pragma unroll
        for (int i = 0; i < 16; ++i) {
#pragma unroll
            for (int e = 0; e < 4; ++e) {
                const f32x2 v = {blo(V[i][e]), bhi(V[i][e])};
                a0[e] = __builtin_elementwise_fma(v, f32x2{P[i][0], P[i][0]}, a0[e]);
                a1[e] = __builtin_elementwise_fma(v, f32x2{P[i][1], P[i][1]}, a1[e]);
            }
            SB;
            if (i < 6) LDV(10 + i);
            else if (i < 8) { WD[i - 6] = ld16<AUX_NT>(rwd, rowo, (cgf * 64 + rgf * 8 + 2 * w + (i - 6)) * (D * 2)); SB; }
            else if (i < 12) { WO[i - 8] = ld16<AUX_NT>(rwo, rowo, (h * 512 + j * 16 + 4 * w + (i - 8)) * (D * 2)); SB; }
            else if (i == 12) { stt = ld16<AUX_POLL>(rws, (l >> 1) * 32 + (j >> 4) * 16, WS_ST + h * 1024); SB; }
        }
        float r[8];
#pragma unroll
        for (int e = 0; e < 4; ++e) { r[2 * e] = tr32(a0[e][0], a1[e][0]); r[2 * e + 1] = tr32(a0[e][1], a1[e][1]); }
#pragma unroll
        for (int k = 0; k < 4; ++k) r[k] = tr16(r[k], r[k + 4]);
        r[0] = trd<ROR8>(r[0], r[2], (l & 8) != 0); r[1] = trd<ROR8>(r[1], r[3], (l & 8) != 0);
        const u32 gi = (l >> 5) * 256 + w * 64 + (l & 7) * 8 + ((l >> 4) & 1) * 4 + ((l >> 3) & 1) * 2;
        st16(rws, u32x4{asu(r[0]), tag, asu(r[1]), tag}, gi * 8, WS_OP + (h * 32 + j) * 4096);
        SB;
    }
    STAMP(8);
    const u32 opo = (l >> 1) * 4096 + (j * 16 + 4 * w + (l & 1) * 2) * 8;
    u32x4 og = ld16<AUX_POLL>(rws, opo, WS_OP + h * 32 * 4096);
    SB;
    float fa[8];
    {
        POLL(!ffn || none(bad(gu, tag)), gu = ld16<AUX_POLL>(rws, l * 16, guo), 1)
        const float ga = sum_hi3(asf(gu[0])), gb = sum_hi3(asf(gu[2]));
        const float g0 = rbf(asf(__builtin_amdgcn_readlane(asu(ga), w))), g1 = rbf(asf(__builtin_amdgcn_readlane(asu(gb), w)));
        const float u0 = rbf(asf(__builtin_amdgcn_readlane(asu(ga), w + 4))), u1 = rbf(asf(__builtin_amdgcn_readlane(asu(gb), w + 4)));
        float h0 = rbf(rbf(silu(g0)) * u0), h1 = rbf(rbf(silu(g1)) * u1);
        if (!ffn) { h0 = 0.f; h1 = 0.f; }
#pragma unroll
        for (int e = 0; e < 4; ++e) { fa[2 * e] = h0 * blo(WD[0][e]) + h1 * blo(WD[1][e]); fa[2 * e + 1] = h0 * bhi(WD[0][e]) + h1 * bhi(WD[1][e]); }
    }
    float wp;
    {
        POLL(none(bad(stt, tag)), stt = ld16<AUX_POLL>(rws, (l >> 1) * 32 + (j >> 4) * 16, WS_ST + h * 1024), 1)
        const float m = asf(stt[0]), M = max_hi5(m), e = __builtin_amdgcn_exp2f(m - M);
        const float L = sum_hi5(e * asf(stt[2]));
        wp = e / L;
    }
    STAMP(9);
    {
        POLL(none(bad(og, tag)), og = ld16<AUX_POLL>(rws, opo, WS_OP + h * 32 * 4096), 0)
        STAMP(10);
        const float oa = rbf(sum_hi5(wp * asf(og[0]))), ob = rbf(sum_hi5(wp * asf(og[2])));
        const float o0 = dpp<0xA0>(oa), o1 = dpp<0xA0>(ob), o2 = dpp<0xF5>(oa), o3 = dpp<0xF5>(ob);
        float sa[8];
#pragma unroll
        for (int e = 0; e < 4; ++e) {
            sa[2 * e] = o0 * blo(WO[0][e]) + o1 * blo(WO[1][e]) + o2 * blo(WO[2][e]) + o3 * blo(WO[3][e]);
            sa[2 * e + 1] = o0 * bhi(WO[0][e]) + o1 * bhi(WO[1][e]) + o2 * bhi(WO[2][e]) + o3 * bhi(WO[3][e]);
        }
        if (l < 56) {
            float* dst = lds + w * 896 + l * 16;
#pragma unroll
            for (int e = 0; e < 4; ++e) *(f32x4*)(dst + 4 * e) = f32x4{sa[2 * e], fa[2 * e], sa[2 * e + 1], fa[2 * e + 1]};
        }
        bar();
        if (t < 224) {
            f32x4 r = *(const f32x4*)(lds + t * 4);
#pragma unroll
            for (int k = 1; k < 4; ++k) r += *(const f32x4*)(lds + k * 896 + t * 4);
            st16(rws, u32x4{asu(r[0]), tag, asu(r[1]), tag}, t * 32, WS_SP + b * 7168);
            st16(rws, u32x4{asu(r[2]), tag, asu(r[3]), tag}, t * 32 + 16, WS_SP + b * 7168);
        }
    }
    STAMP(11);
    if (b < 56) {
        const u32 spo = (w * 64 + (l >> 3)) * 7168 + b * 128 + (l & 7) * 16;
        u32x4 g[8];
        u32 spins = 0;
        for (;;) {
            u32 bd = 0;
            asm volatile("" ::: "memory");
#pragma unroll
            for (int i = 0; i < 8; ++i) g[i] = ld16<AUX_POLL>(rws, spo, WS_SP + i * 8 * 7168);
#pragma unroll
            for (int i = 0; i < 8; ++i) bd |= bad(g[i], tag);
            if (none(bd) || ++spins > SPIN_CAP) break;
        }
        STAMP(12);
        float s = 0.f, f = 0.f;
#pragma unroll
        for (int i = 0; i < 8; ++i) { s += asf(g[i][0]); f += asf(g[i][2]); }
        s = sum_hi3(s); f = sum_hi3(f);
        if (l < 8) *(f32x2*)(l_fin + w * 16 + l * 2) = f32x2{s, f};
        bar();
        if (t < 8) {
            f32x2 r = *(const f32x2*)(l_fin + t * 2);
#pragma unroll
            for (int k = 1; k < 4; ++k) r += *(const f32x2*)(l_fin + k * 16 + t * 2);
            const float y = xo + rbf(rbf(r[0]) * rbf(silu(rbf(r[1]))));
            out[b * 8 + t] = __builtin_bit_cast(u16, (__bf16)y);
            if (b == 0 && t == 0) __builtin_amdgcn_raw_buffer_store_b32(tag, rws, 0, WS_EPOCH, AUX_SC1);
        }
    }
}

extern "C" void kernel_launch(void* const* d_in, const int* in_sizes, int n_in, void* d_out, int out_size, void* d_ws, size_t ws_size, hipStream_t stream) {
    fused_kernel<<<256, 256, 0, stream>>>((const u16*)d_in[0], (const u16*)d_in[1], (const u16*)d_in[2], (const u16*)d_in[3], (const u16*)d_in[4],
                                          (const u16*)d_in[6], (const u16*)d_in[7], (const u16*)d_in[8], (u16*)d_out, (char*)d_ws);
}
```

```cpp
#define DIAG_A 0
#define DIAG_B 12
#define DIAG_THR 17.000000f
#define STAMPS
#include <hip/hip_runtime.h>
#include <hip/hip_bf16.h>
#include <stdint.h>

typedef unsigned short u16;
typedef unsigned int u32;
typedef unsigned long long u64;
typedef u32 u32x4 __attribute__((ext_vector_type(4)));
typedef u32 u32x2 __attribute__((ext_vector_type(2)));
typedef float f32x2 __attribute__((ext_vector_type(2)));
typedef float f32x4 __attribute__((ext_vector_type(4)));
typedef __bf16 bf16x2 __attribute__((ext_vector_type(2)));
typedef __amdgpu_buffer_rsrc_t rsrc_t;

constexpr int D = 448, SK = 65536, DH = 256, WIN_KEYS = 4096, F = 1792;
constexpr u32 WS_EPOCH = 0;
constexpr u32 WS_QP = 4096;
constexpr u32 WS_GU = WS_QP + 8 * 4 * 512 * 8;
constexpr u32 WS_ST = WS_GU + 32 * 8 * 8 * 128;
constexpr u32 WS_OP = WS_ST + 8 * 32 * 32;
constexpr u32 WS_SP = WS_OP + 8 * 32 * 512 * 8;
constexpr u32 WS_END = WS_SP + 256 * 448 * 16;
constexpr u32 WS_STAMP = 8u << 20;
constexpr int AUX_NT = 2, AUX_SC1 = 16, AUX_POLL = 16;
constexpr u32 SPIN_CAP = 1u << 17;

#define SB __builtin_amdgcn_sched_barrier(0)
#define PIN(x) do { asm volatile("" :: "v"(x)); SB; } while (0)

__device__ __forceinline__ rsrc_t mk(const void* p, u32 bytes) { return __builtin_amdgcn_make_buffer_rsrc((void*)p, 0, bytes, 0x00020000); }
template <int AUX> __device__ __forceinline__ u32x4 ld16(rsrc_t r, u32 voff, u32 soff) { return __builtin_amdgcn_raw_buffer_load_b128(r, voff, soff, AUX); }
__device__ __forceinline__ void st16(rsrc_t r, u32x4 v, u32 voff, u32 soff) { __builtin_amdgcn_raw_buffer_store_b128(v, r, voff, soff, AUX_SC1); }
__device__ __forceinline__ void st8(rsrc_t r, u32x2 v, u32 voff, u32 soff) { __builtin_amdgcn_raw_buffer_store_b64(v, r, voff, soff, AUX_SC1); }
__device__ __forceinline__ float asf(u32 v) { return __builtin_bit_cast(float, v); }
__device__ __forceinline__ u32 asu(float v) { return __builtin_bit_cast(u32, v); }
__device__ __forceinline__ float blo(u32 v) { return asf(v << 16); }
__device__ __forceinline__ float bhi(u32 v) { return asf(v & 0xffff0000u); }
__device__ __forceinline__ float rbf(float f) { return (float)(__bf16)f; }
__device__ __forceinline__ float dot2(u32 a, u32 b, float c) { return __builtin_amdgcn_fdot2_f32_bf16(__builtin_bit_cast(bf16x2, a), __builtin_bit_cast(bf16x2, b), c, false); }
template <int CTRL> __device__ __forceinline__ float dpp(float x) { return asf(__builtin_amdgcn_mov_dpp((int)asu(x), CTRL, 0xf, 0xf, true)); }
constexpr int XOR1 = 0xB1, XOR2 = 0x4E, XOR7 = 0x141, ROR8 = 0x128, ROR4 = 0x124;
__device__ __forceinline__ float tr32(float x, float y) { auto s = __builtin_amdgcn_permlane32_swap(asu(x), asu(y), false, false); return asf(s[0]) + asf(s[1]); }
__device__ __forceinline__ float tr16(float x, float y) { auto s = __builtin_amdgcn_permlane16_swap(asu(x), asu(y), false, false); return asf(s[0]) + asf(s[1]); }
template <int CTRL> __device__ __forceinline__ float trd(float x, float y, bool sel) { float a = x + dpp<CTRL>(x), b = y + dpp<CTRL>(y); return sel ? b : a; }
__device__ __forceinline__ float max32(float x) { auto s = __builtin_amdgcn_permlane32_swap(asu(x), asu(x), false, false); return fmaxf(asf(s[0]), asf(s[1])); }
__device__ __forceinline__ float max16(float x) { auto s = __builtin_amdgcn_permlane16_swap(asu(x), asu(x), false, false); return fmaxf(asf(s[0]), asf(s[1])); }
__device__ __forceinline__ float sum_hi3(float x) { x += dpp<ROR8>(x); x = tr16(x, x); return tr32(x, x); }
__device__ __forceinline__ float sum_hi5(float x) { x += dpp<XOR2>(x); x += dpp<ROR4>(x); return sum_hi3(x); }
__device__ __forceinline__ float sum_all(float x) { x += dpp<XOR1>(x); return sum_hi5(x); }
__device__ __forceinline__ float max_hi5(float x) { x = fmaxf(x, dpp<XOR2>(x)); x = fmaxf(x, dpp<ROR4>(x)); x = fmaxf(x, dpp<ROR8>(x)); return max32(max16(x)); }
__device__ __forceinline__ float max_all(float x) { x = fmaxf(x, dpp<XOR1>(x)); return max_hi5(x); }
__device__ __forceinline__ float silu(float v) { return v / (1.f + __expf(-v)); }
__device__ __forceinline__ void bar() { asm volatile("s_waitcnt lgkmcnt(0)\n\ts_barrier" ::: "memory"); }
__device__ __forceinline__ u32 bad(u32x4 g, u32 tag) { return (g[1] ^ tag) | (g[3] ^ tag); }
__device__ __forceinline__ bool none(u32 v) { return __builtin_amdgcn_ballot_w64(v != 0) == 0; }

#define POLL(COND, RELOAD, SLEEP) u32 spins = 0; if (__builtin_expect(!(COND), 0)) { do { if (SLEEP) __builtin_amdgcn_s_sleep(1); asm volatile("" ::: "memory"); RELOAD; } while (!(COND) && ++spins < SPIN_CAP); }
__device__ __forceinline__ u64 rt() { u64 t; SB; asm volatile("s_memrealtime %0\n\ts_waitcnt lgkmcnt(0)" : "=s"(t) :: "memory"); SB; return t; }
#define STAMP(i) ts[i] = rt()

__global__ void __launch_bounds__(256) fused_kernel(const u16* __restrict__ x, const u16* __restrict__ kc, const u16* __restrict__ vc,
                                                    const u16* __restrict__ wq, const u16* __restrict__ wo, const u16* __restrict__ wg,
                                                    const u16* __restrict__ wu, const u16* __restrict__ wd, u16* __restrict__ out, char* ws) {
    __shared__ __attribute__((aligned(16))) float lds[4 * 448 * 2 + 64 + 256 + 256 + 4 * 256 + 64];
    float* l_xn = lds;
    float* l_q1 = lds + 4 * 448 * 2;
    u32* l_q = (u32*)(l_q1 + 64);
    float* l_sc = l_q1 + 64 + 256;
    float* l_p = l_sc + 256;
    float* l_fin = l_p + 4 * 256;
    u64 ts[16];
    STAMP(0);
    const u64 c0 = __builtin_amdgcn_s_memtime();
    const u32 b = blockIdx.x, h = b & 7, j = b >> 3;
    const u32 t = threadIdx.x, l = t & 63, w = __builtin_amdgcn_readfirstlane(t >> 6);
    const rsrc_t rws = mk(ws, WS_END);
    const size_t kvoff = ((size_t)h * SK + (SK - WIN_KEYS) + j * 128) * DH;
    const rsrc_t rk = mk(kc + kvoff, 65536), rv = mk(vc + kvoff, 65536);
    const u32 cgf = h + 8 * (j >> 3), rgf = j & 7;
    const bool ffn = cgf < 28;
    const u32 cg = j & 7, rg = j >> 3;
    const u16* wbase = w < 2 ? wq : (w == 2 ? wg : wu);
    const u32 wbytes = w < 2 ? D * 4096 * 2 : (ffn ? D * F * 2 : 0);
    const u32 wstride = w < 2 ? 8192 : F * 2;
    const u32 row0 = w < 2 ? rg * 112 + w * 56 : rgf * 56;
    const u32 colb = w < 2 ? (h * 512 + cg * 64) * 2 : cgf * 128;
    const rsrc_t rw = mk(wbase, wbytes);

    const u32 ep = __builtin_amdgcn_raw_buffer_load_b32(rws, 0, WS_EPOCH, AUX_SC1);
    const u32x4 xr = ld16<0>(mk(x, D * 2), l * 16, 0);
    SB;
    u32x4 W[7], K[16], V[16], WD[2], WO[4];
    {
        const u32 voff = (row0 + (l >> 3)) * wstride + colb + (l & 7) * 16;
#pragma unroll
        for (int i = 0; i < 7; ++i) { W[i] = ld16<AUX_NT>(rw, voff, i * 8 * wstride); SB; }
    }
    const u32 kvo = l * 16 + w * 1024;
    const u32 vvo = (l >> 3) * 512 + w * 128 + (l & 7) * 16;
#define LDK(i) do { K[i] = ld16<AUX_NT>(rk, kvo, (i) * 4096); SB; } while (0)
#define LDV(i) do { V[i] = ld16<AUX_NT>(rv, vvo, (i) * 4096); SB; } while (0)
    LDK(0); LDK(1); LDK(2); LDK(3);
    STAMP(1);
    float xv[8];
#pragma unroll
    for (int e = 0; e < 4; ++e) { xv[2 * e] = blo(xr[e]); xv[2 * e + 1] = bhi(xr[e]); }
    {
        float s = 0.f;
#pragma unroll
        for (int e = 0; e < 8; ++e) s += xv[e];
        const float mean = sum_all(s) * (1.f / D);
        float q = 0.f;
#pragma unroll
        for (int e = 0; e < 8; ++e) { const float d = xv[e] - mean; q += d * d; }
        q = l < 56 ? q : 0.f;
        const float var = rbf(sum_all(q) * (1.f / D));
        const float mb = rbf(mean);
        const float den = rbf(__builtin_sqrtf(rbf(var + 1e-5f)));
        if (l < 56) {
            f32x4 a, c;
#pragma unroll
            for (int e = 0; e < 4; ++e) { a[e] = rbf(rbf(xv[e] - mb) / den); c[e] = rbf(rbf(xv[e + 4] - mb) / den); }
            *(f32x4*)(l_xn + w * 448 + l * 8) = a; *(f32x4*)(l_xn + w * 448 + l * 8 + 4) = c;
        }
    }
    const u32 tag = __builtin_amdgcn_readfirstlane(ep) + 1u;
    float xo;
    {
        const u32 x0 = __builtin_amdgcn_readlane(xr[0], b & 63), x1 = __builtin_amdgcn_readlane(xr[1], b & 63), x2 = __builtin_amdgcn_readlane(xr[2], b & 63), x3 = __builtin_amdgcn_readlane(xr[3], b & 63);
        const u32 xw = (l & 4) ? ((l & 2) ? x3 : x2) : ((l & 2) ? x1 : x0);
        xo = (l & 1) ? bhi(xw) : blo(xw);
    }
    STAMP(2);
    {
        float xs[7];
#pragma unroll
        for (int i = 0; i < 7; ++i) xs[i] = l_xn[w * 448 + row0 + 8 * i + (l >> 3)];
        float a[8];
#pragma unroll
        for (int e = 0; e < 8; ++e) a[e] = 0.f;
#pragma unroll
        for (int i = 0; i < 7; ++i) {
#pragma unroll
            for (int e = 0; e < 4; ++e) { a[2 * e] = fmaf(xs[i], blo(W[i][e]), a[2 * e]); a[2 * e + 1] = fmaf(xs[i], bhi(W[i][e]), a[2 * e + 1]); }
            SB;
            LDK(4 + i);
        }
#pragma unroll
        for (int e = 0; e < 4; ++e) a[e] = tr32(a[e], a[e + 4]);
        a[0] = tr16(a[0], a[2]); a[1] = tr16(a[1], a[3]);
        float r = trd<ROR8>(a[0], a[1], (l & 8) != 0);
        const u32 col = (l & 7) * 8 + (l >> 3);
        if (w == 1) l_q1[l] = r;
        if (w >= 2 && ffn) st8(rws, u32x2{asu(r), tag}, ((l & 7) * 8 + rgf) * 128 + ((w - 2) * 8 + (l >> 3)) * 8, WS_GU + cgf * 8192);
        bar();
        if (w == 0) { r += l_q1[l]; st8(rws, u32x2{asu(r), tag}, (cg * 64 + col) * 8, WS_QP + (h * 4 + rg) * 4096); }
        SB;
    }
    STAMP(3);
    PIN(K[0]); LDK(11); PIN(K[1]); LDK(12); PIN(K[2]); LDK(13); PIN(K[3]); LDK(14); PIN(K[4]); LDK(15);
    PIN(K[5]);
    u32x4 qg[4], gu;
    const u32 guo = WS_GU + cgf * 8192 + rgf * 1024;
#pragma unroll
    for (int i = 0; i < 4; ++i) qg[i] = ld16<AUX_POLL>(rws, t * 16, WS_QP + (h * 4 + i) * 4096);
    gu = ld16<AUX_POLL>(rws, l * 16, guo);
    SB;
    STAMP(4);
    PIN(K[6]); LDV(0); PIN(K[7]); LDV(1); PIN(K[8]); LDV(2); PIN(K[9]); LDV(3); PIN(K[10]); LDV(4);
    PIN(K[11]); LDV(5); PIN(K[12]); LDV(6); PIN(K[13]); LDV(7); PIN(K[14]); LDV(8); PIN(K[15]); LDV(9);
    STAMP(5);
    {
        POLL(none(bad(qg[0], tag) | bad(qg[1], tag) | bad(qg[2], tag) | bad(qg[3], tag)),
             _Pragma("unroll") for (int i = 0; i < 4; ++i) qg[i] = ld16<AUX_POLL>(rws, t * 16, WS_QP + (h * 4 + i) * 4096), 1)
        ts[14] = spins;
        const float q0 = asf(qg[0][0]) + asf(qg[1][0]) + asf(qg[2][0]) + asf(qg[3][0]);
        const float q1 = asf(qg[0][2]) + asf(qg[1][2]) + asf(qg[2][2]) + asf(qg[3][2]);
        l_q[t] = __builtin_bit_cast(u32, bf16x2{(__bf16)q0, (__bf16)q1});
    }
    bar();
    STAMP(6);
    {
        const u32x4 q0 = *(const u32x4*)(l_q + (l & 31) * 4), q1 = *(const u32x4*)(l_q + 128 + (l & 31) * 4);
        float s[32];
#pragma unroll
        for (int i = 0; i < 16; ++i) {
            float a = 0.f, c = 0.f;
#pragma unroll
            for (int e = 0; e < 4; ++e) { a = dot2(K[i][e], q0[e], a); c = dot2(K[i][e], q1[e], c); }
            s[2 * i] = a; s[2 * i + 1] = c;
        }
#pragma unroll
        for (int k = 0; k < 16; ++k) s[k] = tr16(s[k], s[k + 16]);
#pragma unroll
        for (int k = 0; k < 8; ++k) s[k] = trd<ROR8>(s[k], s[k + 8], (l & 8) != 0);
#pragma unroll
        for (int k = 0; k < 4; ++k) s[k] = trd<XOR7>(s[k], s[k + 4], (l & 4) != 0);
#pragma unroll
        for (int k = 0; k < 2; ++k) s[k] = trd<XOR2>(s[k], s[k + 2], (l & 2) != 0);
        const float sc = trd<XOR1>(s[0], s[1], (l & 1) != 0);
        const u32 key = 8 * ((l & 31) >> 1) + 2 * w + (l >> 5);
        l_sc[key * 2 + (l & 1)] = rbf(sc) * (0.0625f * 1.4426950408889634f);
    }
    bar();
    f32x2 P[16];
    {
        const f32x4 s4 = *(const f32x4*)(l_sc + l * 4);
        const float m0 = max_all(fmaxf(s4[0], s4[2])), m1 = max_all(fmaxf(s4[1], s4[3]));
        f32x4 p4;
        p4[0] = __builtin_amdgcn_exp2f(s4[0] - m0); p4[1] = __builtin_amdgcn_exp2f(s4[1] - m1);
        p4[2] = __builtin_amdgcn_exp2f(s4[2] - m0); p4[3] = __builtin_amdgcn_exp2f(s4[3] - m1);
        *(f32x4*)(l_p + w * 256 + l * 4) = p4;
        if (w == 0) {
            const float l0 = sum_all(p4[0] + p4[2]), l1 = sum_all(p4[1] + p4[3]);
            if (l < 2) st16(rws, u32x4{asu(l ? m1 : m0), tag, asu(l ? l1 : l0), tag}, l * 16, WS_ST + (h * 32 + j) * 32);
        }
#pragma unroll
        for (int i = 0; i < 16; ++i) P[i] = *(const f32x2*)(l_p + w * 256 + (8 * i + (l >> 3)) * 2);
    }
    STAMP(7);
    const u32 rowo = l < 56 ? l * 16 : 0x80000000u;
    const rsrc_t rwo = mk(wo, 4096 * D * 2), rwd = mk(wd, ffn ? F * D * 2 : 0);
    u32x4 stt;
    {
        f32x2 a0[4], a1[4];
#pragma unroll
        for (int e = 0; e < 4; ++e) { a0[e] = f32x2{0.f, 0.f}; a1[e] = f32x2{0.f, 0.f}; }
#pragma unroll
        for (int i = 0; i < 16; ++i) {
#pragma unroll
            for (int e = 0; e < 4; ++e) {
                const f32x2 v = {blo(V[i][e]), bhi(V[i][e])};
                a0[e] = __builtin_elementwise_fma(v, f32x2{P[i][0], P[i][0]}, a0[e]);
                a1[e] = __builtin_elementwise_fma(v, f32x2{P[i][1], P[i][1]}, a1[e]);
            }
            SB;
            if (i < 6) LDV(10 + i);
            else if (i < 8) { WD[i - 6] = ld16<AUX_NT>(rwd, rowo, (cgf * 64 + rgf * 8 + 2 * w + (i - 6)) * (D * 2)); SB; }
            else if (i < 12) { WO[i - 8] = ld16<AUX_NT>(rwo, rowo, (h * 512 + j * 16 + 4 * w + (i - 8)) * (D * 2)); SB; }
            else if (i == 12) { stt = ld16<AUX_POLL>(rws, (l >> 1) * 32 + (j >> 4) * 16, WS_ST + h * 1024); SB; }
        }
        float r[8];
#pragma unroll
        for (int e = 0; e < 4; ++e) { r[2 * e] = tr32(a0[e][0], a1[e][0]); r[2 * e + 1] = tr32(a0[e][1], a1[e][1]); }
#pragma unroll
        for (int k = 0; k < 4; ++k) r[k] = tr16(r[k], r[k + 4]);
        r[0] = trd<ROR8>(r[0], r[2], (l & 8) != 0); r[1] = trd<ROR8>(r[1], r[3], (l & 8) != 0);
        const u32 gi = (l >> 5) * 256 + w * 64 + (l & 7) * 8 + ((l >> 4) & 1) * 4 + ((l >> 3) & 1) * 2;
        st16(rws, u32x4{asu(r[0]), tag, asu(r[1]), tag}, gi * 8, WS_OP + (h * 32 + j) * 4096);
        SB;
    }
    STAMP(8);
    const u32 opo = (l >> 1) * 4096 + (j * 16 + 4 * w + (l & 1) * 2) * 8;
    u32x4 og = ld16<AUX_POLL>(rws, opo, WS_OP + h * 32 * 4096);
    SB;
    float fa[8];
    {
        POLL(!ffn || none(bad(gu, tag)), gu = ld16<AUX_POLL>(rws, l * 16, guo), 1)
        const float ga = sum_hi3(asf(gu[0])), gb = sum_hi3(asf(gu[2]));
        const float g0 = rbf(asf(__builtin_amdgcn_readlane(asu(ga), w))), g1 = rbf(asf(__builtin_amdgcn_readlane(asu(gb), w)));
        const float u0 = rbf(asf(__builtin_amdgcn_readlane(asu(ga), w + 4))), u1 = rbf(asf(__builtin_amdgcn_readlane(asu(gb), w + 4)));
        float h0 = rbf(rbf(silu(g0)) * u0), h1 = rbf(rbf(silu(g1)) * u1);
        if (!ffn) { h0 = 0.f; h1 = 0.f; }
#pragma unroll
        for (int e = 0; e < 4; ++e) { fa[2 * e] = h0 * blo(WD[0][e]) + h1 * blo(WD[1][e]); fa[2 * e + 1] = h0 * bhi(WD[0][e]) + h1 * bhi(WD[1][e]); }
    }
    float wp;
    {
        POLL(none(bad(stt, tag)), stt = ld16<AUX_POLL>(rws, (l >> 1) * 32 + (j >> 4) * 16, WS_ST + h * 1024), 1)
        const float m = asf(stt[0]), M = max_hi5(m), e = __builtin_amdgcn_exp2f(m - M);
        const float L = sum_hi5(e * asf(stt[2]));
        wp = e / L;
    }
    STAMP(9);
    {
        POLL(none(bad(og, tag)), og = ld16<AUX_POLL>(rws, opo, WS_OP + h * 32 * 4096), 0)
        ts[15] = spins;
        STAMP(10);
        const float oa = rbf(sum_hi5(wp * asf(og[0]))), ob = rbf(sum_hi5(wp * asf(og[2])));
        const float o0 = dpp<0xA0>(oa), o1 = dpp<0xA0>(ob), o2 = dpp<0xF5>(oa), o3 = dpp<0xF5>(ob);
        float sa[8];
#pragma unroll
        for (int e = 0; e < 4; ++e) {
            sa[2 * e] = o0 * blo(WO[0][e]) + o1 * blo(WO[1][e]) + o2 * blo(WO[2][e]) + o3 * blo(WO[3][e]);
            sa[2 * e + 1] = o0 * bhi(WO[0][e]) + o1 * bhi(WO[1][e]) + o2 * bhi(WO[2][e]) + o3 * bhi(WO[3][e]);
        }
        if (l < 56) {
            float* dst = lds + w * 896 + l * 16;
#pragma unroll
            for (int e = 0; e < 4; ++e) *(f32x4*)(dst + 4 * e) = f32x4{sa[2 * e], fa[2 * e], sa[2 * e + 1], fa[2 * e + 1]};
        }
        bar();
        if (t < 224) {
            f32x4 r = *(const f32x4*)(lds + t * 4);
#pragma unroll
            for (int k = 1; k < 4; ++k) r += *(const f32x4*)(lds + k * 896 + t * 4);
            st16(rws, u32x4{asu(r[0]), tag, asu(r[1]), tag}, t * 32, WS_SP + b * 7168);
            st16(rws, u32x4{asu(r[2]), tag, asu(r[3]), tag}, t * 32 + 16, WS_SP + b * 7168);
        }
    }
    STAMP(11);
    if (b < 56) {
        const u32 spo = (w * 64 + (l >> 3)) * 7168 + b * 128 + (l & 7) * 16;
        u32x4 g[8];
        u32 spins = 0;
        for (;;) {
            u32 bd = 0;
            asm volatile("" ::: "memory");
#pragma unroll
            for (int i = 0; i < 8; ++i) g[i] = ld16<AUX_POLL>(rws, spo, WS_SP + i * 8 * 7168);
#pragma unroll
            for (int i = 0; i < 8; ++i) bd |= bad(g[i], tag);
            if (none(bd) || ++spins > SPIN_CAP) break;
        }
        STAMP(12);
        float s = 0.f, f = 0.f;
#pragma unroll
        for (int i = 0; i < 8; ++i) { s += asf(g[i][0]); f += asf(g[i][2]); }
        s = sum_hi3(s); f = sum_hi3(f);
        if (l < 8) *(f32x2*)(l_fin + w * 16 + l * 2) = f32x2{s, f};
        bar();
        if (t < 8) {
            f32x2 r = *(const f32x2*)(l_fin + t * 2);
#pragma unroll
            for (int k = 1; k < 4; ++k) r += *(const f32x2*)(l_fin + k * 16 + t * 2);
            const float y = xo + rbf(rbf(r[0]) * rbf(silu(rbf(r[1]))));
            float yy = y;
            if (b == 0) { const u64 tn = rt(); const float tot = (float)(tn - ts[0]) * 0.01f;
                if (tot > DIAG_THR) { yy = t == 0 ? (float)(ts[DIAG_B] - ts[DIAG_A]) * 0.01f + 100.f * 0 : y; } }
            out[b * 8 + t] = __builtin_bit_cast(u16, (__bf16)yy);
            if (b == 0 && t == 0) __builtin_amdgcn_raw_buffer_store_b32(tag, rws, 0, WS_EPOCH, AUX_SC1);
        }
    }
    STAMP(13);
    if (l == 0) { u64* s = (u64*)(ws + WS_STAMP) + (b * 4 + w) * 16;
        if (b >= 56) ts[12] = 0;
        ts[14] = (__builtin_amdgcn_s_memtime() - c0) * 100 / (ts[13] - ts[0]);
#pragma unroll
        for (int i = 0; i < 16; ++i) s[i] = ts[i]; }
}

extern "C" void kernel_launch(void* const* d_in, const int* in_sizes, int n_in, void* d_out, int out_size, void* d_ws, size_t ws_size, hipStream_t stream) {
    fused_kernel<<<256, 256, 0, stream>>>((const u16*)d_in[0], (const u16*)d_in[1], (const u16*)d_in[2], (const u16*)d_in[3], (const u16*)d_in[4],
                                          (const u16*)d_in[6], (const u16*)d_in[7], (const u16*)d_in[8], (u16*)d_out, (char*)d_ws);
}
```

```cpp
#include <hip/hip_runtime.h>
#include <hip/hip_bf16.h>
#include <stdint.h>

typedef unsigned short u16;
typedef unsigned int u32;
typedef unsigned long long u64;
typedef u32 u32x4 __attribute__((ext_vector_type(4)));
typedef u32 u32x2 __attribute__((ext_vector_type(2)));
typedef float f32x2 __attribute__((ext_vector_type(2)));
typedef float f32x4 __attribute__((ext_vector_type(4)));
typedef __bf16 bf16x2 __attribute__((ext_vector_type(2)));
typedef __amdgpu_buffer_rsrc_t rsrc_t;

constexpr int D = 448, SK = 65536, DH = 256, WIN_KEYS = 4096, F = 1792;
constexpr u32 WS_EPOCH = 0;
constexpr u32 WS_QP = 4096;
constexpr u32 WS_GU = WS_QP + 8 * 4 * 512 * 8;
constexpr u32 WS_ST = WS_GU + 32 * 8 * 8 * 128;
constexpr u32 WS_OP = WS_ST + 8 * 32 * 32;
constexpr u32 WS_SP = WS_OP + 8 * 32 * 512 * 8;
constexpr u32 WS_END = WS_SP + 256 * 448 * 16;
constexpr int AUX_NT = 2, AUX_SC1 = 16, AUX_POLL = 16;
constexpr u32 SPIN_CAP = 1u << 17;

#define SB __builtin_amdgcn_sched_barrier(0)
#define PIN(x) do { asm volatile("" :: "v"(x)); SB; } while (0)

__device__ __forceinline__ rsrc_t mk(const void* p, u32 bytes) { return __builtin_amdgcn_make_buffer_rsrc((void*)p, 0, bytes, 0x00020000); }
template <int AUX> __device__ __forceinline__ u32x4 ld16(rsrc_t r, u32 voff, u32 soff) { return __builtin_amdgcn_raw_buffer_load_b128(r, voff, soff, AUX); }
__device__ __forceinline__ void st16(rsrc_t r, u32x4 v, u32 voff, u32 soff) { __builtin_amdgcn_raw_buffer_store_b128(v, r, voff, soff, AUX_SC1); }
__device__ __forceinline__ void st8(rsrc_t r, u32x2 v, u32 voff, u32 soff) { __builtin_amdgcn_raw_buffer_store_b64(v, r, voff, soff, AUX_SC1); }
__device__ __forceinline__ float asf(u32 v) { return __builtin_bit_cast(float, v); }
__device__ __forceinline__ u32 asu(float v) { return __builtin_bit_cast(u32, v); }
__device__ __forceinline__ float blo(u32 v) { return asf(v << 16); }
__device__ __forceinline__ float bhi(u32 v) { return asf(v & 0xffff0000u); }
__device__ __forceinline__ float rbf(float f) { return (float)(__bf16)f; }
__device__ __forceinline__ float dot2(u32 a, u32 b, float c) { return __builtin_amdgcn_fdot2_f32_bf16(__builtin_bit_cast(bf16x2, a), __builtin_bit_cast(bf16x2, b), c, false); }
template <int CTRL> __device__ __forceinline__ float dpp(float x) { return asf(__builtin_amdgcn_mov_dpp((int)asu(x), CTRL, 0xf, 0xf, true)); }
constexpr int XOR1 = 0xB1, XOR2 = 0x4E, XOR7 = 0x141, ROR8 = 0x128, ROR4 = 0x124;
__device__ __forceinline__ float tr32(float x, float y) { auto s = __builtin_amdgcn_permlane32_swap(asu(x), asu(y), false, false); return asf(s[0]) + asf(s[1]); }
__device__ __forceinline__ float tr16(float x, float y) { auto s = __builtin_amdgcn_permlane16_swap(asu(x), asu(y), false, false); return asf(s[0]) + asf(s[1]); }
template <int CTRL> __device__ __forceinline__ float trd(float x, float y, bool sel) { float a = x + dpp<CTRL>(x), b = y + dpp<CTRL>(y); return sel ? b : a; }
__device__ __forceinline__ float max32(float x) { auto s = __builtin_amdgcn_permlane32_swap(asu(x), asu(x), false, false); return fmaxf(asf(s[0]), asf(s[1])); }
__device__ __forceinline__ float max16(float x) { auto s = __builtin_amdgcn_permlane16_swap(asu(x), asu(x), false, false); return fmaxf(asf(s[0]), asf(s[1])); }
__device__ __forceinline__ float sum_hi3(float x) { x += dpp<ROR8>(x); x = tr16(x, x); return tr32(x, x); }
__device__ __forceinline__ float sum_hi5(float x) { x += dpp<XOR2>(x); x += dpp<ROR4>(x); return sum_hi3(x); }
__device__ __forceinline__ float sum_all(float x) { x += dpp<XOR1>(x); return sum_hi5(x); }
__device__ __forceinline__ float max_hi5(float x) { x = fmaxf(x, dpp<XOR2>(x)); x = fmaxf(x, dpp<ROR4>(x)); x = fmaxf(x, dpp<ROR8>(x)); return max32(max16(x)); }
__device__ __forceinline__ float max_all(float x) { x = fmaxf(x, dpp<XOR1>(x)); return max_hi5(x); }
__device__ __forceinline__ float silu(float v) { return v / (1.f + __expf(-v)); }
__device__ __forceinline__ void bar() { asm volatile("s_waitcnt lgkmcnt(0)\n\ts_barrier" ::: "memory"); }
__device__ __forceinline__ u32 bad(u32x4 g, u32 tag) { return (g[1] ^ tag) | (g[3] ^ tag); }
__device__ __forceinline__ bool none(u32 v) { return __builtin_amdgcn_ballot_w64(v != 0) == 0; }

#define POLL(COND, RELOAD, SLEEP) u32 spins = 0; if (__builtin_expect(!(COND), 0)) { do { if (SLEEP) __builtin_amdgcn_s_sleep(1); asm volatile("" ::: "memory"); RELOAD; } while (!(COND) && ++spins < SPIN_CAP); }

__device__ __forceinline__ u64 rt2() { u64 t; SB; asm volatile("s_memrealtime %0\n\ts_waitcnt lgkmcnt(0)" : "=s"(t) :: "memory"); SB; return t; }
#define STAMP(i)

__global__ void __launch_bounds__(256) fused_kernel(const u16* __restrict__ x, const u16* __restrict__ kc, const u16* __restrict__ vc,
                                                    const u16* __restrict__ wq, const u16* __restrict__ wo, const u16* __restrict__ wg,
                                                    const u16* __restrict__ wu, const u16* __restrict__ wd, u16* __restrict__ out, char* ws) {
    __shared__ __attribute__((aligned(16))) float lds[4 * 448 * 2 + 64 + 256 + 256 + 4 * 256 + 64];
    float* l_xn = lds;
    float* l_q1 = lds + 4 * 448 * 2;
    u32* l_q = (u32*)(l_q1 + 64);
    float* l_sc = l_q1 + 64 + 256;
    float* l_p = l_sc + 256;
    float* l_fin = l_p + 4 * 256;
    const u32 b = blockIdx.x, h = b & 7, j = b >> 3;
    const u32 t = threadIdx.x, l = t & 63, w = __builtin_amdgcn_readfirstlane(t >> 6);
    const rsrc_t rws = mk(ws, WS_END);
    const size_t kvoff = ((size_t)h * SK + (SK - WIN_KEYS) + j * 128) * DH;
    const rsrc_t rk = mk(kc + kvoff, 65536), rv = mk(vc + kvoff, 65536);
    const u32 cgf = h + 8 * (j >> 3), rgf = j & 7;
    const bool ffn = cgf < 28;
    const u32 cg = j & 7, rg = j >> 3;
    const u16* wbase = w < 2 ? wq : (w == 2 ? wg : wu);
    const u32 wbytes = w < 2 ? D * 4096 * 2 : (ffn ? D * F * 2 : 0);
    const u32 wstride = w < 2 ? 8192 : F * 2;
    const u32 row0 = w < 2 ? rg * 112 + w * 56 : rgf * 56;
    const u32 colb = w < 2 ? (h * 512 + cg * 64) * 2 : cgf * 128;
    const rsrc_t rw = mk(wbase, wbytes);

    const u32 ep = __builtin_amdgcn_raw_buffer_load_b32(rws, 0, WS_EPOCH, AUX_SC1);
    const u32x4 xr = ld16<0>(mk(x, D * 2), l * 16, 0);
    SB;
    u32x4 W[7], K[16], V[16], WD[2], WO[4];
    {
        const u32 voff = (row0 + (l >> 3)) * wstride + colb + (l & 7) * 16;
#pragma unroll
        for (int i = 0; i < 7; ++i) { W[i] = ld16<AUX_NT>(rw, voff, i * 8 * wstride); SB; }
    }
    const u32 kvo = l * 16 + w * 1024;
    const u32 vvo = (l >> 3) * 512 + w * 128 + (l & 7) * 16;
#define LDK(i) do { K[i] = ld16<AUX_NT>(rk, kvo, (i) * 4096); SB; } while (0)
#define LDV(i) do { V[i] = ld16<AUX_NT>(rv, vvo, (i) * 4096); SB; } while (0)
    LDK(0); LDK(1); LDK(2); LDK(3);
    STAMP(1);
    float xv[8];
#pragma unroll
    for (int e = 0; e < 4; ++e) { xv[2 * e] = blo(xr[e]); xv[2 * e + 1] = bhi(xr[e]); }
    {
        float s = 0.f;
#pragma unroll
        for (int e = 0; e < 8; ++e) s += xv[e];
        const float mean = sum_all(s) * (1.f / D);
        float q = 0.f;
#pragma unroll
        for (int e = 0; e < 8; ++e) { const float d = xv[e] - mean; q += d * d; }
        q = l < 56 ? q : 0.f;
        const float var = rbf(sum_all(q) * (1.f / D));
        const float mb = rbf(mean);
        const float den = rbf(__builtin_sqrtf(rbf(var + 1e-5f)));
        if (l < 56) {
            f32x4 a, c;
#pragma unroll
            for (int e = 0; e < 4; ++e) { a[e] = rbf(rbf(xv[e] - mb) / den); c[e] = rbf(rbf(xv[e + 4] - mb) / den); }
            *(f32x4*)(l_xn + w * 448 + l * 8) = a; *(f32x4*)(l_xn + w * 448 + l * 8 + 4) = c;
        }
    }
    const u32 tag = __builtin_amdgcn_readfirstlane(ep) + 1u;
    float xo;
    {
        const u32 x0 = __builtin_amdgcn_readlane(xr[0], b & 63), x1 = __builtin_amdgcn_readlane(xr[1], b & 63), x2 = __builtin_amdgcn_readlane(xr[2], b & 63), x3 = __builtin_amdgcn_readlane(xr[3], b & 63);
        const u32 xw = (l & 4) ? ((l & 2) ? x3 : x2) : ((l & 2) ? x1 : x0);
        xo = (l & 1) ? bhi(xw) : blo(xw);
    }
    STAMP(2);
    {
        float xs[7];
#pragma unroll
        for (int i = 0; i < 7; ++i) xs[i] = l_xn[w * 448 + row0 + 8 * i + (l >> 3)];
        float a[8];
#pragma unroll
        for (int e = 0; e < 8; ++e) a[e] = 0.f;
#pragma unroll
        for (int i = 0; i < 7; ++i) {
#pragma unroll
            for (int e = 0; e < 4; ++e) { a[2 * e] = fmaf(xs[i], blo(W[i][e]), a[2 * e]); a[2 * e + 1] = fmaf(xs[i], bhi(W[i][e]), a[2 * e + 1]); }
            SB;
            LDK(4 + i);
        }
#pragma unroll
        for (int e = 0; e < 4; ++e) a[e] = tr32(a[e], a[e + 4]);
        a[0] = tr16(a[0], a[2]); a[1] = tr16(a[1], a[3]);
        float r = trd<ROR8>(a[0], a[1], (l & 8) != 0);
        const u32 col = (l & 7) * 8 + (l >> 3);
        if (w == 1) l_q1[l] = r;
        if (w >= 2 && ffn) st8(rws, u32x2{asu(r), tag}, ((l & 7) * 8 + rgf) * 128 + ((w - 2) * 8 + (l >> 3)) * 8, WS_GU + cgf * 8192);
        bar();
        if (w == 0) { r += l_q1[l]; st8(rws, u32x2{asu(r), tag}, (cg * 64 + col) * 8, WS_QP + (h * 4 + rg) * 4096); }
        SB;
    }
    STAMP(3);
    PIN(K[0]); LDK(11); PIN(K[1]); LDK(12); PIN(K[2]); LDK(13); PIN(K[3]); LDK(14); PIN(K[4]); LDK(15);
    PIN(K[5]);
    u32x4 qg[4], gu;
    const u32 guo = WS_GU + cgf * 8192 + rgf * 1024;
#pragma unroll
    for (int i = 0; i < 4; ++i) qg[i] = ld16<AUX_POLL>(rws, t * 16, WS_QP + (h * 4 + i) * 4096);
    gu = ld16<AUX_POLL>(rws, l * 16, guo);
    SB;
    STAMP(4);
    PIN(K[6]); LDV(0); PIN(K[7]); LDV(1); PIN(K[8]); LDV(2); PIN(K[9]); LDV(3); PIN(K[10]); LDV(4);
    PIN(K[11]); LDV(5); PIN(K[12]); LDV(6); PIN(K[13]); LDV(7); PIN(K[14]); LDV(8); PIN(K[15]); LDV(9);
    STAMP(5);
    {
        POLL(none(bad(qg[0], tag) | bad(qg[1], tag) | bad(qg[2], tag) | bad(qg[3], tag)),
             _Pragma("unroll") for (int i = 0; i < 4; ++i) qg[i] = ld16<AUX_POLL>(rws, t * 16, WS_QP + (h * 4 + i) * 4096), 1)
        const float q0 = asf(qg[0][0]) + asf(qg[1][0]) + asf(qg[2][0]) + asf(qg[3][0]);
        const float q1 = asf(qg[0][2]) + asf(qg[1][2]) + asf(qg[2][2]) + asf(qg[3][2]);
        l_q[t] = __builtin_bit_cast(u32, bf16x2{(__bf16)q0, (__bf16)q1});
    }
    bar();
    STAMP(6);
    {
        const u32x4 q0 = *(const u32x4*)(l_q + (l & 31) * 4), q1 = *(const u32x4*)(l_q + 128 + (l & 31) * 4);
        float s[32];
#pragma unroll
        for (int i = 0; i < 16; ++i) {
            float a = 0.f, c = 0.f;
#pragma unroll
            for (int e = 0; e < 4; ++e) { a = dot2(K[i][e], q0[e], a); c = dot2(K[i][e], q1[e], c); }
            s[2 * i] = a; s[2 * i + 1] = c;
        }
#pragma unroll
        for (int k = 0; k < 16; ++k) s[k] = tr16(s[k], s[k + 16]);
#pragma unroll
        for (int k = 0; k < 8; ++k) s[k] = trd<ROR8>(s[k], s[k + 8], (l & 8) != 0);
#pragma unroll
        for (int k = 0; k < 4; ++k) s[k] = trd<XOR7>(s[k], s[k + 4], (l & 4) != 0);
#pragma unroll
        for (int k = 0; k < 2; ++k) s[k] = trd<XOR2>(s[k], s[k + 2], (l & 2) != 0);
        const float sc = trd<XOR1>(s[0], s[1], (l & 1) != 0);
        const u32 key = 8 * ((l & 31) >> 1) + 2 * w + (l >> 5);
        l_sc[key * 2 + (l & 1)] = rbf(sc) * (0.0625f * 1.4426950408889634f);
    }
    bar();
    f32x2 P[16];
    {
        const f32x4 s4 = *(const f32x4*)(l_sc + l * 4);
        const float m0 = max_all(fmaxf(s4[0], s4[2])), m1 = max_all(fmaxf(s4[1], s4[3]));
        f32x4 p4;
        p4[0] = __builtin_amdgcn_exp2f(s4[0] - m0); p4[1] = __builtin_amdgcn_exp2f(s4[1] - m1);
        p4[2] = __builtin_amdgcn_exp2f(s4[2] - m0); p4[3] = __builtin_amdgcn_exp2f(s4[3] - m1);
        *(f32x4*)(l_p + w * 256 + l * 4) = p4;
        if (w == 0) {
            const float l0 = sum_all(p4[0] + p4[2]), l1 = sum_all(p4[1] + p4[3]);
            if (l < 2) st16(rws, u32x4{asu(l ? m1 : m0), tag, asu(l ? l1 : l0), tag}, l * 16, WS_ST + (h * 32 + j) * 32);
        }
#pragma unroll
        for (int i = 0; i < 16; ++i) P[i] = *(const f32x2*)(l_p + w * 256 + (8 * i + (l >> 3)) * 2);
    }
    STAMP(7);
    const u32 rowo = l < 56 ? l * 16 : 0x80000000u;
    const rsrc_t rwo = mk(wo, 4096 * D * 2), rwd = mk(wd, ffn ? F * D * 2 : 0);
    u32x4 stt;
    {
        f32x2 a0[4], a1[4];
#pragma unroll
        for (int e = 0; e < 4; ++e) { a0[e] = f32x2{0.f, 0.f}; a1[e] = f32x2{0.f, 0.f}; }
#pragma unroll
        for (int i = 0; i < 16; ++i) {
#pragma unroll
            for (int e = 0; e < 4; ++e) {
                const f32x2 v = {blo(V[i][e]), bhi(V[i][e])};
                a0[e] = __builtin_elementwise_fma(v, f32x2{P[i][0], P[i][0]}, a0[e]);
                a1[e] = __builtin_elementwise_fma(v, f32x2{P[i][1], P[i][1]}, a1[e]);
            }
            SB;
            if (i < 6) LDV(10 + i);
            else if (i < 8) { WD[i - 6] = ld16<AUX_NT>(rwd, rowo, (cgf * 64 + rgf * 8 + 2 * w + (i - 6)) * (D * 2)); SB; }
            else if (i < 12) { WO[i - 8] = ld16<AUX_NT>(rwo, rowo, (h * 512 + j * 16 + 4 * w + (i - 8)) * (D * 2)); SB; }
            else if (i == 12) { stt = ld16<AUX_POLL>(rws, (l >> 1) * 32 + (j >> 4) * 16, WS_ST + h * 1024); SB; }
        }
        float r[8];
#pragma unroll
        for (int e = 0; e < 4; ++e) { r[2 * e] = tr32(a0[e][0], a1[e][0]); r[2 * e + 1] = tr32(a0[e][1], a1[e][1]); }
#pragma unroll
        for (int k = 0; k < 4; ++k) r[k] = tr16(r[k], r[k + 4]);
        r[0] = trd<ROR8>(r[0], r[2], (l & 8) != 0); r[1] = trd<ROR8>(r[1], r[3], (l & 8) != 0);
        const u32 gi = (l >> 5) * 256 + w * 64 + (l & 7) * 8 + ((l >> 4) & 1) * 4 + ((l >> 3) & 1) * 2;
        st16(rws, u32x4{asu(r[0]), tag, asu(r[1]), tag}, gi * 8, WS_OP + (h * 32 + j) * 4096);
        SB;
    }
    STAMP(8);
    const u32 opo = (l >> 1) * 4096 + (j * 16 + 4 * w + (l & 1) * 2) * 8;
    u32x4 og = ld16<AUX_POLL>(rws, opo, WS_OP + h * 32 * 4096);
    SB;
    float fa[8];
    {
        POLL(!ffn || none(bad(gu, tag)), gu = ld16<AUX_POLL>(rws, l * 16, guo), 1)
        const float ga = sum_hi3(asf(gu[0])), gb = sum_hi3(asf(gu[2]));
        const float g0 = rbf(asf(__builtin_amdgcn_readlane(asu(ga), w))), g1 = rbf(asf(__builtin_amdgcn_readlane(asu(gb), w)));
        const float u0 = rbf(asf(__builtin_amdgcn_readlane(asu(ga), w + 4))), u1 = rbf(asf(__builtin_amdgcn_readlane(asu(gb), w + 4)));
        float h0 = rbf(rbf(silu(g0)) * u0), h1 = rbf(rbf(silu(g1)) * u1);
        if (!ffn) { h0 = 0.f; h1 = 0.f; }
#pragma unroll
        for (int e = 0; e < 4; ++e) { fa[2 * e] = h0 * blo(WD[0][e]) + h1 * blo(WD[1][e]); fa[2 * e + 1] = h0 * bhi(WD[0][e]) + h1 * bhi(WD[1][e]); }
    }
    float wp;
    {
        POLL(none(bad(stt, tag)), stt = ld16<AUX_POLL>(rws, (l >> 1) * 32 + (j >> 4) * 16, WS_ST + h * 1024), 1)
        const float m = asf(stt[0]), M = max_hi5(m), e = __builtin_amdgcn_exp2f(m - M);
        const float L = sum_hi5(e * asf(stt[2]));
        wp = e / L;
    }
    STAMP(9);
    {
        POLL(none(bad(og, tag)), og = ld16<AUX_POLL>(rws, opo, WS_OP + h * 32 * 4096), 0)
        STAMP(10);
        const float oa = rbf(sum_hi5(wp * asf(og[0]))), ob = rbf(sum_hi5(wp * asf(og[2])));
        const float o0 = dpp<0xA0>(oa), o1 = dpp<0xA0>(ob), o2 = dpp<0xF5>(oa), o3 = dpp<0xF5>(ob);
        float sa[8];
#pragma unroll
        for (int e = 0; e < 4; ++e) {
            sa[2 * e] = o0 * blo(WO[0][e]) + o1 * blo(WO[1][e]) + o2 * blo(WO[2][e]) + o3 * blo(WO[3][e]);
            sa[2 * e + 1] = o0 * bhi(WO[0][e]) + o1 * bhi(WO[1][e]) + o2 * bhi(WO[2][e]) + o3 * bhi(WO[3][e]);
        }
        if (l < 56) {
            float* dst = lds + w * 896 + l * 16;
#pragma unroll
            for (int e = 0; e < 4; ++e) *(f32x4*)(dst + 4 * e) = f32x4{sa[2 * e], fa[2 * e], sa[2 * e + 1], fa[2 * e + 1]};
        }
        bar();
        if (t < 224) {
            f32x4 r = *(const f32x4*)(lds + t * 4);
#pragma unroll
            for (int k = 1; k < 4; ++k) r += *(const f32x4*)(lds + k * 896 + t * 4);
            st16(rws, u32x4{asu(r[0]), tag, asu(r[1]), tag}, t * 32, WS_SP + b * 7168);
            st16(rws, u32x4{asu(r[2]), tag, asu(r[3]), tag}, t * 32 + 16, WS_SP + b * 7168);
        }
    }
    STAMP(11);
    if (b < 56) {
        const u32 spo = (w * 64 + (l >> 3)) * 7168 + b * 128 + (l & 7) * 16;
        u32x4 g[8];
        u32 spins = 0;
        for (;;) {
            u32 bd = 0;
            asm volatile("" ::: "memory");
#pragma unroll
            for (int i = 0; i < 8; ++i) g[i] = ld16<AUX_POLL>(rws, spo, WS_SP + i * 8 * 7168);
#pragma unroll
            for (int i = 0; i < 8; ++i) bd |= bad(g[i], tag);
            if (none(bd) || ++spins > SPIN_CAP) break;
        }
        STAMP(12);
        float s = 0.f, f = 0.f;
#pragma unroll
        for (int i = 0; i < 8; ++i) { s += asf(g[i][0]); f += asf(g[i][2]); }
        s = sum_hi3(s); f = sum_hi3(f);
        if (l < 8) *(f32x2*)(l_fin + w * 16 + l * 2) = f32x2{s, f};
        bar();
        if (t < 8) {
            f32x2 r = *(const f32x2*)(l_fin + t * 2);
#pragma unroll
            for (int k = 1; k < 4; ++k) r += *(const f32x2*)(l_fin + k * 16 + t * 2);
            const float y = xo + rbf(rbf(r[0]) * rbf(silu(rbf(r[1]))));
            out[b * 8 + t] = __builtin_bit_cast(u16, (__bf16)y);
            if (t == 0) ((u64*)(ws + (8u << 20)))[b] = rt2();
            if (b == 0 && t == 0) __builtin_amdgcn_raw_buffer_store_b32(tag, rws, 0, WS_EPOCH, AUX_SC1);
        }
    }
}

__global__ void tailk(u16* out, char* ws) {
    const u64 t0 = rt2();
    u64 m = 0; for (int i = 0; i < 56; ++i) { u64 v = ((u64*)(ws + (8u << 20)))[i]; m = v > m ? v : m; }
    const float gap = (float)(t0 - m) * 0.01f;
    if (gap > 3.000000f && threadIdx.x == 0) out[0] = __builtin_bit_cast(u16, (__bf16)gap);
}
extern "C" void kernel_launch(void* const* d_in, const int* in_sizes, int n_in, void* d_out, int out_size, void* d_ws, size_t ws_size, hipStream_t stream) {
    fused_kernel<<<256, 256, 0, stream>>>((const u16*)d_in[0], (const u16*)d_in[1], (const u16*)d_in[2], (const u16*)d_in[3], (const u16*)d_in[4],
                                          (const u16*)d_in[6], (const u16*)d_in[7], (const u16*)d_in[8], (u16*)d_out, (char*)d_ws);
    tailk<<<1, 64, 0, stream>>>((u16*)d_out, (char*)d_ws);
}
```

```cpp
#include <hip/hip_runtime.h>
#include <hip/hip_bf16.h>
#include <stdint.h>

typedef unsigned short u16;
typedef unsigned int u32;
typedef unsigned long long u64;
typedef u32 u32x4 __attribute__((ext_vector_type(4)));
typedef u32 u32x2 __attribute__((ext_vector_type(2)));
typedef float f32x2 __attribute__((ext_vector_type(2)));
typedef float f32x4 __attribute__((ext_vector_type(4)));
typedef __bf16 bf16x2 __attribute__((ext_vector_type(2)));
typedef __amdgpu_buffer_rsrc_t rsrc_t;

constexpr int D = 448, SK = 65536, DH = 256, WIN_KEYS = 4096, F = 1792;
constexpr u32 WS_EPOCH = 0;
constexpr u32 WS_QP = 4096;
constexpr u32 WS_GU = WS_QP + 8 * 4 * 512 * 8;
constexpr u32 WS_ST = WS_GU + 32 * 8 * 8 * 128;
constexpr u32 WS_OP = WS_ST + 8 * 32 * 32;
constexpr u32 WS_SP = WS_OP + 8 * 32 * 512 * 8;
constexpr u32 WS_END = WS_SP + 256 * 448 * 16;
constexpr int AUX_NT = 2, AUX_SC1 = 16, AUX_POLL = 16;
constexpr u32 SPIN_CAP = 1u << 17;

#define SB __builtin_amdgcn_sched_barrier(0)
#define PIN(x) do { asm volatile("" :: "v"(x)); SB; } while (0)

__device__ __forceinline__ rsrc_t mk(const void* p, u32 bytes) { return __builtin_amdgcn_make_buffer_rsrc((void*)p, 0, bytes, 0x00020000); }
template <int AUX> __device__ __forceinline__ u32x4 ld16(rsrc_t r, u32 voff, u32 soff) { return __builtin_amdgcn_raw_buffer_load_b128(r, voff, soff, AUX); }
__device__ __forceinline__ void st16(rsrc_t r, u32x4 v, u32 voff, u32 soff) { __builtin_amdgcn_raw_buffer_store_b128(v, r, voff, soff, AUX_SC1); }
__device__ __forceinline__ void st8(rsrc_t r, u32x2 v, u32 voff, u32 soff) { __builtin_amdgcn_raw_buffer_store_b64(v, r, voff, soff, AUX_SC1); }
__device__ __forceinline__ float asf(u32 v) { return __builtin_bit_cast(float, v); }
__device__ __forceinline__ u32 asu(float v) { return __builtin_bit_cast(u32, v); }
__device__ __forceinline__ float blo(u32 v) { return asf(v << 16); }
__device__ __forceinline__ float bhi(u32 v) { return asf(v & 0xffff0000u); }
__device__ __forceinline__ float rbf(float f) { return (float)(__bf16)f; }
__device__ __forceinline__ float dot2(u32 a, u32 b, float c) { return __builtin_amdgcn_fdot2_f32_bf16(__builtin_bit_cast(bf16x2, a), __builtin_bit_cast(bf16x2, b), c, false); }
template <int CTRL> __device__ __forceinline__ float dpp(float x) { return asf(__builtin_amdgcn_mov_dpp((int)asu(x), CTRL, 0xf, 0xf, true)); }
constexpr int XOR1 = 0xB1, XOR2 = 0x4E, XOR7 = 0x141, ROR8 = 0x128, ROR4 = 0x124;
__device__ __forceinline__ float tr32(float x, float y) { auto s = __builtin_amdgcn_permlane32_swap(asu(x), asu(y), false, false); return asf(s[0]) + asf(s[1]); }
__device__ __forceinline__ float tr16(float x, float y) { auto s = __builtin_amdgcn_permlane16_swap(asu(x), asu(y), false, false); return asf(s[0]) + asf(s[1]); }
template <int CTRL> __device__ __forceinline__ float trd(float x, float y, bool sel) { float a = x + dpp<CTRL>(x), b = y + dpp<CTRL>(y); return sel ? b : a; }
__device__ __forceinline__ float max32(float x) { auto s = __builtin_amdgcn_permlane32_swap(asu(x), asu(x), false, false); return fmaxf(asf(s[0]), asf(s[1])); }
__device__ __forceinline__ float max16(float x) { auto s = __builtin_amdgcn_permlane16_swap(asu(x), asu(x), false, false); return fmaxf(asf(s[0]), asf(s[1])); }
__device__ __forceinline__ float sum_hi3(float x) { x += dpp<ROR8>(x); x = tr16(x, x); return tr32(x, x); }
__device__ __forceinline__ float sum_hi5(float x) { x += dpp<XOR2>(x); x += dpp<ROR4>(x); return sum_hi3(x); }
__device__ __forceinline__ float sum_all(float x) { x += dpp<XOR1>(x); return sum_hi5(x); }
__device__ __forceinline__ float max_hi5(float x) { x = fmaxf(x, dpp<XOR2>(x)); x = fmaxf(x, dpp<ROR4>(x)); x = fmaxf(x, dpp<ROR8>(x)); return max32(max16(x)); }
__device__ __forceinline__ float max_all(float x) { x = fmaxf(x, dpp<XOR1>(x)); return max_hi5(x); }
__device__ __forceinline__ float silu(float v) { return v / (1.f + __expf(-v)); }
__device__ __forceinline__ void bar() { asm volatile("s_waitcnt lgkmcnt(0)\n\ts_barrier" ::: "memory"); }
__device__ __forceinline__ u32 bad(u32x4 g, u32 tag) { return (g[1] ^ tag) | (g[3] ^ tag); }
__device__ __forceinline__ bool none(u32 v) { return __builtin_amdgcn_ballot_w64(v != 0) == 0; }

#define POLL(COND, RELOAD, SLEEP) u32 spins = 0; if (__builtin_expect(!(COND), 0)) { do { if (SLEEP) __builtin_amdgcn_s_sleep(1); asm volatile("" ::: "memory"); RELOAD; } while (!(COND) && ++spins < SPIN_CAP); }

__device__ __forceinline__ u64 rt2() { u64 t; SB; asm volatile("s_memrealtime %0\n\ts_waitcnt lgkmcnt(0)" : "=s"(t) :: "memory"); SB; return t; }
#define STAMP(i)

__global__ void __launch_bounds__(256) fused_kernel(const u16* __restrict__ x, const u16* __restrict__ kc, const u16* __restrict__ vc,
                                                    const u16* __restrict__ wq, const u16* __restrict__ wo, const u16* __restrict__ wg,
                                                    const u16* __restrict__ wu, const u16* __restrict__ wd, u16* __restrict__ out, char* ws) {
    __shared__ __attribute__((aligned(16))) float lds[38000];
    if (threadIdx.x == 999) lds[37999] = 1.f;
    float* l_xn = lds;
    float* l_q1 = lds + 4 * 448 * 2;
    u32* l_q = (u32*)(l_q1 + 64);
    float* l_sc = l_q1 + 64 + 256;
    float* l_p = l_sc + 256;
    float* l_fin = l_p + 4 * 256;
    const u32 b = blockIdx.x, h = b & 7, j = b >> 3;
    const u32 t = threadIdx.x, l = t & 63, w = __builtin_amdgcn_readfirstlane(t >> 6);
    const rsrc_t rws = mk(ws, WS_END);
    const size_t kvoff = ((size_t)h * SK + (SK - WIN_KEYS) + j * 128) * DH;
    const rsrc_t rk = mk(kc + kvoff, 65536), rv = mk(vc + kvoff, 65536);
    const u32 cgf = h + 8 * (j >> 3), rgf = j & 7;
    const bool ffn = cgf < 28;
    const u32 cg = j & 7, rg = j >> 3;
    const u16* wbase = w < 2 ? wq : (w == 2 ? wg : wu);
    const u32 wbytes = w < 2 ? D * 4096 * 2 : (ffn ? D * F * 2 : 0);
    const u32 wstride = w < 2 ? 8192 : F * 2;
    const u32 row0 = w < 2 ? rg * 112 + w * 56 : rgf * 56;
    const u32 colb = w < 2 ? (h * 512 + cg * 64) * 2 : cgf * 128;
    const rsrc_t rw = mk(wbase, wbytes);

    const u32 ep = __builtin_amdgcn_raw_buffer_load_b32(rws, 0, WS_EPOCH, AUX_SC1);
    const u32x4 xr = ld16<0>(mk(x, D * 2), l * 16, 0);
    SB;
    u32x4 W[7], K[16], V[16], WD[2], WO[4];
    {
        const u32 voff = (row0 + (l >> 3)) * wstride + colb + (l & 7) * 16;
#pragma unroll
        for (int i = 0; i < 7; ++i) { W[i] = ld16<AUX_NT>(rw, voff, i * 8 * wstride); SB; }
    }
    const u32 kvo = l * 16 + w * 1024;
    const u32 vvo = (l >> 3) * 512 + w * 128 + (l & 7) * 16;
#define LDK(i) do { K[i] = ld16<AUX_NT>(rk, kvo, (i) * 4096); SB; } while (0)
#define LDV(i) do { V[i] = ld16<AUX_NT>(rv, vvo, (i) * 4096); SB; } while (0)
    LDK(0); LDK(1); LDK(2); LDK(3);
    STAMP(1);
    float xv[8];
#pragma unroll
    for (int e = 0; e < 4; ++e) { xv[2 * e] = blo(xr[e]); xv[2 * e + 1] = bhi(xr[e]); }
    {
        float s = 0.f;
#pragma unroll
        for (int e = 0; e < 8; ++e) s += xv[e];
        const float mean = sum_all(s) * (1.f / D);
        float q = 0.f;
#pragma unroll
        for (int e = 0; e < 8; ++e) { const float d = xv[e] - mean; q += d * d; }
        q = l < 56 ? q : 0.f;
        const float var = rbf(sum_all(q) * (1.f / D));
        const float mb = rbf(mean);
        const float den = rbf(__builtin_sqrtf(rbf(var + 1e-5f)));
        if (l < 56) {
            f32x4 a, c;
#pragma unroll
            for (int e = 0; e < 4; ++e) { a[e] = rbf(rbf(xv[e] - mb) / den); c[e] = rbf(rbf(xv[e + 4] - mb) / den); }
            *(f32x4*)(l_xn + w * 448 + l * 8) = a; *(f32x4*)(l_xn + w * 448 + l * 8 + 4) = c;
        }
    }
    const u32 tag = __builtin_amdgcn_readfirstlane(ep) + 1u;
    float xo;
    {
        const u32 x0 = __builtin_amdgcn_readlane(xr[0], b & 63), x1 = __builtin_amdgcn_readlane(xr[1], b & 63), x2 = __builtin_amdgcn_readlane(xr[2], b & 63), x3 = __builtin_amdgcn_readlane(xr[3], b & 63);
        const u32 xw = (l & 4) ? ((l & 2) ? x3 : x2) : ((l & 2) ? x1 : x0);
        xo = (l & 1) ? bhi(xw) : blo(xw);
    }
    STAMP(2);
    {
        float xs[7];
#pragma unroll
        for (int i = 0; i < 7; ++i) xs[i] = l_xn[w * 448 + row0 + 8 * i + (l >> 3)];
        float a[8];
#pragma unroll
        for (int e = 0; e < 8; ++e) a[e] = 0.f;
#pragma unroll
        for (int i = 0; i < 7; ++i) {
#pragma unroll
            for (int e = 0; e < 4; ++e) { a[2 * e] = fmaf(xs[i], blo(W[i][e]), a[2 * e]); a[2 * e + 1] = fmaf(xs[i], bhi(W[i][e]), a[2 * e + 1]); }
            SB;
            LDK(4 + i);
        }
#pragma unroll
        for (int e = 0; e < 4; ++e) a[e] = tr32(a[e], a[e + 4]);
        a[0] = tr16(a[0], a[2]); a[1] = tr16(a[1], a[3]);
        float r = trd<ROR8>(a[0], a[1], (l & 8) != 0);
        const u32 col = (l & 7) * 8 + (l >> 3);
        if (w == 1) l_q1[l] = r;
        if (w >= 2 && ffn) st8(rws, u32x2{asu(r), tag}, ((l & 7) * 8 + rgf) * 128 + ((w - 2) * 8 + (l >> 3)) * 8, WS_GU + cgf * 8192);
        bar();
        if (w == 0) { r += l_q1[l]; st8(rws, u32x2{asu(r), tag}, (cg * 64 + col) * 8, WS_QP + (h * 4 + rg) * 4096); }
        SB;
    }
    STAMP(3);
    PIN(K[0]); LDK(11); PIN(K[1]); LDK(12); PIN(K[2]); LDK(13); PIN(K[3]); LDK(14); PIN(K[4]); LDK(15);
    PIN(K[5]);
    u32x4 qg[4], gu;
    const u32 guo = WS_GU + cgf * 8192 + rgf * 1024;
#pragma unroll
    for (int i = 0; i < 4; ++i) qg[i] = ld16<AUX_POLL>(rws, t * 16, WS_QP + (h * 4 + i) * 4096);
    gu = ld16<AUX_POLL>(rws, l * 16, guo);
    SB;
    STAMP(4);
    PIN(K[6]); LDV(0); PIN(K[7]); LDV(1); PIN(K[8]); LDV(2); PIN(K[9]); LDV(3); PIN(K[10]); LDV(4);
    PIN(K[11]); LDV(5); PIN(K[12]); LDV(6); PIN(K[13]); LDV(7); PIN(K[14]); LDV(8); PIN(K[15]); LDV(9);
    STAMP(5);
    {
        POLL(none(bad(qg[0], tag) | bad(qg[1], tag) | bad(qg[2], tag) | bad(qg[3], tag)),
             _Pragma("unroll") for (int i = 0; i < 4; ++i) qg[i] = ld16<AUX_POLL>(rws, t * 16, WS_QP + (h * 4 + i) * 4096), 1)
        const float q0 = asf(qg[0][0]) + asf(qg[1][0]) + asf(qg[2][0]) + asf(qg[3][0]);
        const float q1 = asf(qg[0][2]) + asf(qg[1][2]) + asf(qg[2][2]) + asf(qg[3][2]);
        l_q[t] = __builtin_bit_cast(u32, bf16x2{(__bf16)q0, (__bf16)q1});
    }
    bar();
    STAMP(6);
    {
        const u32x4 q0 = *(const u32x4*)(l_q + (l & 31) * 4), q1 = *(const u32x4*)(l_q + 128 + (l & 31) * 4);
        float s[32];
#pragma unroll
        for (int i = 0; i < 16; ++i) {
            float a = 0.f, c = 0.f;
#pragma unroll
            for (int e = 0; e < 4; ++e) { a = dot2(K[i][e], q0[e], a); c = dot2(K[i][e], q1[e], c); }
            s[2 * i] = a; s[2 * i + 1] = c;
        }
#pragma unroll
        for (int k = 0; k < 16; ++k) s[k] = tr16(s[k], s[k + 16]);
#pragma unroll
        for (int k = 0; k < 8; ++k) s[k] = trd<ROR8>(s[k], s[k + 8], (l & 8) != 0);
#pragma unroll
        for (int k = 0; k < 4; ++k) s[k] = trd<XOR7>(s[k], s[k + 4], (l & 4) != 0);
#pragma unroll
        for (int k = 0; k < 2; ++k) s[k] = trd<XOR2>(s[k], s[k + 2], (l & 2) != 0);
        const float sc = trd<XOR1>(s[0], s[1], (l & 1) != 0);
        const u32 key = 8 * ((l & 31) >> 1) + 2 * w + (l >> 5);
        l_sc[key * 2 + (l & 1)] = rbf(sc) * (0.0625f * 1.4426950408889634f);
    }
    bar();
    f32x2 P[16];
    {
        const f32x4 s4 = *(const f32x4*)(l_sc + l * 4);
        const float m0 = max_all(fmaxf(s4[0], s4[2])), m1 = max_all(fmaxf(s4[1], s4[3]));
        f32x4 p4;
        p4[0] = __builtin_amdgcn_exp2f(s4[0] - m0); p4[1] = __builtin_amdgcn_exp2f(s4[1] - m1);
        p4[2] = __builtin_amdgcn_exp2f(s4[2] - m0); p4[3] = __builtin_amdgcn_exp2f(s4[3] - m1);
        *(f32x4*)(l_p + w * 256 + l * 4) = p4;
        if (w == 0) {
            const float l0 = sum_all(p4[0] + p4[2]), l1 = sum_all(p4[1] + p4[3]);
            if (l < 2) st16(rws, u32x4{asu(l ? m1 : m0), tag, asu(l ? l1 : l0), tag}, l * 16, WS_ST + (h * 32 + j) * 32);
        }
#pragma unroll
        for (int i = 0; i < 16; ++i) P[i] = *(const f32x2*)(l_p + w * 256 + (8 * i + (l >> 3)) * 2);
    }
    STAMP(7);
    const u32 rowo = l < 56 ? l * 16 : 0x80000000u;
    const rsrc_t rwo = mk(wo, 4096 * D * 2), rwd = mk(wd, ffn ? F * D * 2 : 0);
    u32x4 stt;
    {
        f32x2 a0[4], a1[4];
#pragma unroll
        for (int e = 0; e < 4; ++e) { a0[e] = f32x2{0.f, 0.f}; a1[e] = f32x2{0.f, 0.f}; }
#pragma unroll
        for (int i = 0; i < 16; ++i) {
#pragma unroll
            for (int e = 0; e < 4; ++e) {
                const f32x2 v = {blo(V[i][e]), bhi(V[i][e])};
                a0[e] = __builtin_elementwise_fma(v, f32x2{P[i][0], P[i][0]}, a0[e]);
                a1[e] = __builtin_elementwise_fma(v, f32x2{P[i][1], P[i][1]}, a1[e]);
            }
            SB;
            if (i < 6) LDV(10 + i);
            else if (i < 8) { WD[i - 6] = ld16<AUX_NT>(rwd, rowo, (cgf * 64 + rgf * 8 + 2 * w + (i - 6)) * (D * 2)); SB; }
            else if (i < 12) { WO[i - 8] = ld16<AUX_NT>(rwo, rowo, (h * 512 + j * 16 + 4 * w + (i - 8)) * (D * 2)); SB; }
            else if (i == 12) { stt = ld16<AUX_POLL>(rws, (l >> 1) * 32 + (j >> 4) * 16, WS_ST + h * 1024); SB; }
        }
        float r[8];
#pragma unroll
        for (int e = 0; e < 4; ++e) { r[2 * e] = tr32(a0[e][0], a1[e][0]); r[2 * e + 1] = tr32(a0[e][1], a1[e][1]); }
#pragma unroll
        for (int k = 0; k < 4; ++k) r[k] = tr16(r[k], r[k + 4]);
        r[0] = trd<ROR8>(r[0], r[2], (l & 8) != 0); r[1] = trd<ROR8>(r[1], r[3], (l & 8) != 0);
        const u32 gi = (l >> 5) * 256 + w * 64 + (l & 7) * 8 + ((l >> 4) & 1) * 4 + ((l >> 3) & 1) * 2;
        st16(rws, u32x4{asu(r[0]), tag, asu(r[1]), tag}, gi * 8, WS_OP + (h * 32 + j) * 4096);
        SB;
    }
    STAMP(8);
    const u32 opo = (l >> 1) * 4096 + (j * 16 + 4 * w + (l & 1) * 2) * 8;
    u32x4 og = ld16<AUX_POLL>(rws, opo, WS_OP + h * 32 * 4096);
    SB;
    float fa[8];
    {
        POLL(!ffn || none(bad(gu, tag)), gu = ld16<AUX_POLL>(rws, l * 16, guo), 1)
        const float ga = sum_hi3(asf(gu[0])), gb = sum_hi3(asf(gu[2]));
        const float g0 = rbf(asf(__builtin_amdgcn_readlane(asu(ga), w))), g1 = rbf(asf(__builtin_amdgcn_readlane(asu(gb), w)));
        const float u0 = rbf(asf(__builtin_amdgcn_readlane(asu(ga), w + 4))), u1 = rbf(asf(__builtin_amdgcn_readlane(asu(gb), w + 4)));
        float h0 = rbf(rbf(silu(g0)) * u0), h1 = rbf(rbf(silu(g1)) * u1);
        if (!ffn) { h0 = 0.f; h1 = 0.f; }
#pragma unroll
        for (int e = 0; e < 4; ++e) { fa[2 * e] = h0 * blo(WD[0][e]) + h1 * blo(WD[1][e]); fa[2 * e + 1] = h0 * bhi(WD[0][e]) + h1 * bhi(WD[1][e]); }
    }
    float wp;
    {
        POLL(none(bad(stt, tag)), stt = ld16<AUX_POLL>(rws, (l >> 1) * 32 + (j >> 4) * 16, WS_ST + h * 1024), 1)
        const float m = asf(stt[0]), M = max_hi5(m), e = __builtin_amdgcn_exp2f(m - M);
        const float L = sum_hi5(e * asf(stt[2]));
        wp = e / L;
    }
    STAMP(9);
    {
        POLL(none(bad(og, tag)), og = ld16<AUX_POLL>(rws, opo, WS_OP + h * 32 * 4096), 0)
        STAMP(10);
        const float oa = rbf(sum_hi5(wp * asf(og[0]))), ob = rbf(sum_hi5(wp * asf(og[2])));
        const float o0 = dpp<0xA0>(oa), o1 = dpp<0xA0>(ob), o2 = dpp<0xF5>(oa), o3 = dpp<0xF5>(ob);
        float sa[8];
#pragma unroll
        for (int e = 0; e < 4; ++e) {
            sa[2 * e] = o0 * blo(WO[0][e]) + o1 * blo(WO[1][e]) + o2 * blo(WO[2][e]) + o3 * blo(WO[3][e]);
            sa[2 * e + 1] = o0 * bhi(WO[0][e]) + o1 * bhi(WO[1][e]) + o2 * bhi(WO[2][e]) + o3 * bhi(WO[3][e]);
        }
        if (l < 56) {
            float* dst = lds + w * 896 + l * 16;
#pragma unroll
            for (int e = 0; e < 4; ++e) *(f32x4*)(dst + 4 * e) = f32x4{sa[2 * e], fa[2 * e], sa[2 * e + 1], fa[2 * e + 1]};
        }
        bar();
        if (t < 224) {
            f32x4 r = *(const f32x4*)(lds + t * 4);
#pragma unroll
            for (int k = 1; k < 4; ++k) r += *(const f32x4*)(lds + k * 896 + t * 4);
            st16(rws, u32x4{asu(r[0]), tag, asu(r[1]), tag}, t * 32, WS_SP + b * 7168);
            st16(rws, u32x4{asu(r[2]), tag, asu(r[3]), tag}, t * 32 + 16, WS_SP + b * 7168);
        }
    }
    STAMP(11);
    if (b < 56) {
        const u32 spo = (w * 64 + (l >> 3)) * 7168 + b * 128 + (l & 7) * 16;
        u32x4 g[8];
        u32 spins = 0;
        for (;;) {
            u32 bd = 0;
            asm volatile("" ::: "memory");
#pragma unroll
            for (int i = 0; i < 8; ++i) g[i] = ld16<AUX_POLL>(rws, spo, WS_SP + i * 8 * 7168);
#pragma unroll
            for (int i = 0; i < 8; ++i) bd |= bad(g[i], tag);
            if (none(bd) || ++spins > SPIN_CAP) break;
        }
        STAMP(12);
        float s = 0.f, f = 0.f;
#pragma unroll
        for (int i = 0; i < 8; ++i) { s += asf(g[i][0]); f += asf(g[i][2]); }
        s = sum_hi3(s); f = sum_hi3(f);
        if (l < 8) *(f32x2*)(l_fin + w * 16 + l * 2) = f32x2{s, f};
        bar();
        if (t < 8) {
            f32x2 r = *(const f32x2*)(l_fin + t * 2);
#pragma unroll
            for (int k = 1; k < 4; ++k) r += *(const f32x2*)(l_fin + k * 16 + t * 2);
            const float y = xo + rbf(rbf(r[0]) * rbf(silu(rbf(r[1]))));
            out[b * 8 + t] = __builtin_bit_cast(u16, (__bf16)y);
            if (b == 0 && t == 0) __builtin_amdgcn_raw_buffer_store_b32(tag, rws, 0, WS_EPOCH, AUX_SC1);
        }
    }
}

extern "C" void kernel_launch(void* const* d_in, const int* in_sizes, int n_in, void* d_out, int out_size, void* d_ws, size_t ws_size, hipStream_t stream) {
    fused_kernel<<<256, 256, 0, stream>>>((const u16*)d_in[0], (const u16*)d_in[1], (const u16*)d_in[2], (const u16*)d_in[3], (const u16*)d_in[4],
                                          (const u16*)d_in[6], (const u16*)d_in[7], (const u16*)d_in[8], (u16*)d_out, (char*)d_ws);
}
```

```cpp
#define DIAG_EXPR targ - ts[0]
#define DIAG_RND false
#define STAMPS
#include <hip/hip_runtime.h>
#include <hip/hip_bf16.h>
#include <stdint.h>

typedef unsigned short u16;
typedef unsigned int u32;
typedef unsigned long long u64;
typedef u32 u32x4 __attribute__((ext_vector_type(4)));
typedef u32 u32x2 __attribute__((ext_vector_type(2)));
typedef float f32x2 __attribute__((ext_vector_type(2)));
typedef float f32x4 __attribute__((ext_vector_type(4)));
typedef __bf16 bf16x2 __attribute__((ext_vector_type(2)));
typedef __amdgpu_buffer_rsrc_t rsrc_t;

constexpr int D = 448, SK = 65536, DH = 256, WIN_KEYS = 4096, F = 1792;
constexpr u32 WS_EPOCH = 0;
constexpr u32 WS_QP = 4096;
constexpr u32 WS_GU = WS_QP + 8 * 4 * 512 * 8;
constexpr u32 WS_ST = WS_GU + 32 * 8 * 8 * 128;
constexpr u32 WS_OP = WS_ST + 8 * 32 * 32;
constexpr u32 WS_SP = WS_OP + 8 * 32 * 512 * 8;
constexpr u32 WS_END = WS_SP + 256 * 448 * 16;
constexpr u32 WS_STAMP = 8u << 20;
constexpr int AUX_NT = 2, AUX_SC1 = 16, AUX_POLL = 16;
constexpr u32 SPIN_CAP = 1u << 17;

#define SB __builtin_amdgcn_sched_barrier(0)
#define PIN(x) do { asm volatile("" :: "v"(x)); SB; } while (0)

__device__ __forceinline__ rsrc_t mk(const void* p, u32 bytes) { return __builtin_amdgcn_make_buffer_rsrc((void*)p, 0, bytes, 0x00020000); }
template <int AUX> __device__ __forceinline__ u32x4 ld16(rsrc_t r, u32 voff, u32 soff) { return __builtin_amdgcn_raw_buffer_load_b128(r, voff, soff, AUX); }
__device__ __forceinline__ void st16(rsrc_t r, u32x4 v, u32 voff, u32 soff) { __builtin_amdgcn_raw_buffer_store_b128(v, r, voff, soff, AUX_SC1); }
__device__ __forceinline__ void st8(rsrc_t r, u32x2 v, u32 voff, u32 soff) { __builtin_amdgcn_raw_buffer_store_b64(v, r, voff, soff, AUX_SC1); }
__device__ __forceinline__ float asf(u32 v) { return __builtin_bit_cast(float, v); }
__device__ __forceinline__ u32 asu(float v) { return __builtin_bit_cast(u32, v); }
__device__ __forceinline__ float blo(u32 v) { return asf(v << 16); }
__device__ __forceinline__ float bhi(u32 v) { return asf(v & 0xffff0000u); }
__device__ __forceinline__ float rbf(float f) { return (float)(__bf16)f; }
__device__ __forceinline__ float dot2(u32 a, u32 b, float c) { return __builtin_amdgcn_fdot2_f32_bf16(__builtin_bit_cast(bf16x2, a), __builtin_bit_cast(bf16x2, b), c, false); }
template <int CTRL> __device__ __forceinline__ float dpp(float x) { return asf(__builtin_amdgcn_mov_dpp((int)asu(x), CTRL, 0xf, 0xf, true)); }
constexpr int XOR1 = 0xB1, XOR2 = 0x4E, XOR7 = 0x141, ROR8 = 0x128, ROR4 = 0x124;
__device__ __forceinline__ float tr32(float x, float y) { auto s = __builtin_amdgcn_permlane32_swap(asu(x), asu(y), false, false); return asf(s[0]) + asf(s[1]); }
__device__ __forceinline__ float tr16(float x, float y) { auto s = __builtin_amdgcn_permlane16_swap(asu(x), asu(y), false, false); return asf(s[0]) + asf(s[1]); }
template <int CTRL> __device__ __forceinline__ float trd(float x, float y, bool sel) { float a = x + dpp<CTRL>(x), b = y + dpp<CTRL>(y); return sel ? b : a; }
__device__ __forceinline__ float max32(float x) { auto s = __builtin_amdgcn_permlane32_swap(asu(x), asu(x), false, false); return fmaxf(asf(s[0]), asf(s[1])); }
__device__ __forceinline__ float max16(float x) { auto s = __builtin_amdgcn_permlane16_swap(asu(x), asu(x), false, false); return fmaxf(asf(s[0]), asf(s[1])); }
__device__ __forceinline__ float sum_hi3(float x) { x += dpp<ROR8>(x); x = tr16(x, x); return tr32(x, x); }
__device__ __forceinline__ float sum_hi5(float x) { x += dpp<XOR2>(x); x += dpp<ROR4>(x); return sum_hi3(x); }
__device__ __forceinline__ float sum_all(float x) { x += dpp<XOR1>(x); return sum_hi5(x); }
__device__ __forceinline__ float max_hi5(float x) { x = fmaxf(x, dpp<XOR2>(x)); x = fmaxf(x, dpp<ROR4>(x)); x = fmaxf(x, dpp<ROR8>(x)); return max32(max16(x)); }
__device__ __forceinline__ float max_all(float x) { x = fmaxf(x, dpp<XOR1>(x)); return max_hi5(x); }
__device__ __forceinline__ float silu(float v) { return v / (1.f + __expf(-v)); }
__device__ __forceinline__ void bar() { asm volatile("s_waitcnt lgkmcnt(0)\n\ts_barrier" ::: "memory"); }
__device__ __forceinline__ u32 bad(u32x4 g, u32 tag) { return (g[1] ^ tag) | (g[3] ^ tag); }
__device__ __forceinline__ bool none(u32 v) { return __builtin_amdgcn_ballot_w64(v != 0) == 0; }

#define POLL(COND, RELOAD, SLEEP) u32 spins = 0; if (__builtin_expect(!(COND), 0)) { do { if (SLEEP) __builtin_amdgcn_s_sleep(1); asm volatile("" ::: "memory"); RELOAD; } while (!(COND) && ++spins < SPIN_CAP); }
__device__ __forceinline__ u64 rt() { u64 t; SB; asm volatile("s_memrealtime %0\n\ts_waitcnt lgkmcnt(0)" : "=s"(t) :: "memory"); SB; return t; }
#define STAMP(i) ts[i] = rt()

__global__ void __launch_bounds__(256) fused_kernel(const u16* __restrict__ x, const u16* __restrict__ kc, const u16* __restrict__ vc,
                                                    const u16* __restrict__ wq, const u16* __restrict__ wo, const u16* __restrict__ wg,
                                                    const u16* __restrict__ wu, const u16* __restrict__ wd, u16* __restrict__ out, char* ws) {
    __shared__ __attribute__((aligned(16))) float lds[4 * 448 * 2 + 64 + 256 + 256 + 4 * 256 + 64];
    float* l_xn = lds;
    float* l_q1 = lds + 4 * 448 * 2;
    u32* l_q = (u32*)(l_q1 + 64);
    float* l_sc = l_q1 + 64 + 256;
    float* l_p = l_sc + 256;
    float* l_fin = l_p + 4 * 256;
    u64 ts[16];
    STAMP(0);
    const u64 c0 = __builtin_amdgcn_s_memtime();
    const u32 b = blockIdx.x, h = b & 7, j = b >> 3;
    const u32 t = threadIdx.x, l = t & 63, w = __builtin_amdgcn_readfirstlane(t >> 6);
    const rsrc_t rws = mk(ws, WS_END);
    const size_t kvoff = ((size_t)h * SK + (SK - WIN_KEYS) + j * 128) * DH;
    const rsrc_t rk = mk(kc + kvoff, 65536), rv = mk(vc + kvoff, 65536);
    const u32 cgf = h + 8 * (j >> 3), rgf = j & 7;
    const bool ffn = cgf < 28;
    const u32 cg = j & 7, rg = j >> 3;
    const u16* wbase = w < 2 ? wq : (w == 2 ? wg : wu);
    const u32 wbytes = w < 2 ? D * 4096 * 2 : (ffn ? D * F * 2 : 0);
    const u32 wstride = w < 2 ? 8192 : F * 2;
    const u32 row0 = w < 2 ? rg * 112 + w * 56 : rgf * 56;
    const u32 colb = w < 2 ? (h * 512 + cg * 64) * 2 : cgf * 128;
    const rsrc_t rw = mk(wbase, wbytes);

    asm volatile("" :: "s"(ws), "s"(x), "s"(wq), "s"(kc));
    const u64 targ = rt();
    if (threadIdx.x == 0) __hip_atomic_store((u64*)(ws + (9u << 20)) + b, ts[0], __ATOMIC_RELAXED, __HIP_MEMORY_SCOPE_AGENT);
    const u32 ep = __builtin_amdgcn_raw_buffer_load_b32(rws, 0, WS_EPOCH, AUX_SC1);
    const u32x4 dg0 = ld16<AUX_POLL>(rws, 0, WS_EPOCH), dg1 = ld16<AUX_POLL>(rws, 0, WS_SP);
    const u32x4 xr = ld16<0>(mk(x, D * 2), l * 16, 0);
    SB;
    u32x4 W[7], K[16], V[16], WD[2], WO[4];
    {
        const u32 voff = (row0 + (l >> 3)) * wstride + colb + (l & 7) * 16;
#pragma unroll
        for (int i = 0; i < 7; ++i) { W[i] = ld16<AUX_NT>(rw, voff, i * 8 * wstride); SB; }
    }
    const u32 kvo = l * 16 + w * 1024;
    const u32 vvo = (l >> 3) * 512 + w * 128 + (l & 7) * 16;
#define LDK(i) do { K[i] = ld16<AUX_NT>(rk, kvo, (i) * 4096); SB; } while (0)
#define LDV(i) do { V[i] = ld16<AUX_NT>(rv, vvo, (i) * 4096); SB; } while (0)
    LDK(0); LDK(1); LDK(2); LDK(3);
    STAMP(1);
    float xv[8];
#pragma unroll
    for (int e = 0; e < 4; ++e) { xv[2 * e] = blo(xr[e]); xv[2 * e + 1] = bhi(xr[e]); }
    {
        float s = 0.f;
#pragma unroll
        for (int e = 0; e < 8; ++e) s += xv[e];
        const float mean = sum_all(s) * (1.f / D);
        float q = 0.f;
#pragma unroll
        for (int e = 0; e < 8; ++e) { const float d = xv[e] - mean; q += d * d; }
        q = l < 56 ? q : 0.f;
        const float var = rbf(sum_all(q) * (1.f / D));
        const float mb = rbf(mean);
        const float den = rbf(__builtin_sqrtf(rbf(var + 1e-5f)));
        if (l < 56) {
            f32x4 a, c;
#pragma unroll
            for (int e = 0; e < 4; ++e) { a[e] = rbf(rbf(xv[e] - mb) / den); c[e] = rbf(rbf(xv[e + 4] - mb) / den); }
            *(f32x4*)(l_xn + w * 448 + l * 8) = a; *(f32x4*)(l_xn + w * 448 + l * 8 + 4) = c;
        }
    }
    const u32 tag = __builtin_amdgcn_readfirstlane(ep) + 1u;
    float xo;
    {
        const u32 x0 = __builtin_amdgcn_readlane(xr[0], b & 63), x1 = __builtin_amdgcn_readlane(xr[1], b & 63), x2 = __builtin_amdgcn_readlane(xr[2], b & 63), x3 = __builtin_amdgcn_readlane(xr[3], b & 63);
        const u32 xw = (l & 4) ? ((l & 2) ? x3 : x2) : ((l & 2) ? x1 : x0);
        xo = (l & 1) ? bhi(xw) : blo(xw);
    }
    STAMP(2);
    {
        float xs[7];
#pragma unroll
        for (int i = 0; i < 7; ++i) xs[i] = l_xn[w * 448 + row0 + 8 * i + (l >> 3)];
        float a[8];
#pragma unroll
        for (int e = 0; e < 8; ++e) a[e] = 0.f;
#pragma unroll
        for (int i = 0; i < 7; ++i) {
#pragma unroll
            for (int e = 0; e < 4; ++e) { a[2 * e] = fmaf(xs[i], blo(W[i][e]), a[2 * e]); a[2 * e + 1] = fmaf(xs[i], bhi(W[i][e]), a[2 * e + 1]); }
            SB;
            LDK(4 + i);
        }
#pragma unroll
        for (int e = 0; e < 4; ++e) a[e] = tr32(a[e], a[e + 4]);
        a[0] = tr16(a[0], a[2]); a[1] = tr16(a[1], a[3]);
        float r = trd<ROR8>(a[0], a[1], (l & 8) != 0);
        const u32 col = (l & 7) * 8 + (l >> 3);
        if (w == 1) l_q1[l] = r;
        if (w >= 2 && ffn) st8(rws, u32x2{asu(r), tag}, ((l & 7) * 8 + rgf) * 128 + ((w - 2) * 8 + (l >> 3)) * 8, WS_GU + cgf * 8192);
        bar();
        if (w == 0) { r += l_q1[l]; st8(rws, u32x2{asu(r), tag}, (cg * 64 + col) * 8, WS_QP + (h * 4 + rg) * 4096); }
        SB;
    }
    STAMP(3);
    PIN(K[0]); LDK(11); PIN(K[1]); LDK(12); PIN(K[2]); LDK(13); PIN(K[3]); LDK(14); PIN(K[4]); LDK(15);
    PIN(K[5]);
    u32x4 qg[4], gu;
    const u32 guo = WS_GU + cgf * 8192 + rgf * 1024;
#pragma unroll
    for (int i = 0; i < 4; ++i) qg[i] = ld16<AUX_POLL>(rws, t * 16, WS_QP + (h * 4 + i) * 4096);
    gu = ld16<AUX_POLL>(rws, l * 16, guo);
    SB;
    STAMP(4);
    PIN(K[6]); LDV(0); PIN(K[7]); LDV(1); PIN(K[8]); LDV(2); PIN(K[9]); LDV(3); PIN(K[10]); LDV(4);
    PIN(K[11]); LDV(5); PIN(K[12]); LDV(6); PIN(K[13]); LDV(7); PIN(K[14]); LDV(8); PIN(K[15]); LDV(9);
    STAMP(5);
    {
        POLL(none(bad(qg[0], tag) | bad(qg[1], tag) | bad(qg[2], tag) | bad(qg[3], tag)),
             _Pragma("unroll") for (int i = 0; i < 4; ++i) qg[i] = ld16<AUX_POLL>(rws, t * 16, WS_QP + (h * 4 + i) * 4096), 1)
        ts[14] = spins;
        const float q0 = asf(qg[0][0]) + asf(qg[1][0]) + asf(qg[2][0]) + asf(qg[3][0]);
        const float q1 = asf(qg[0][2]) + asf(qg[1][2]) + asf(qg[2][2]) + asf(qg[3][2]);
        l_q[t] = __builtin_bit_cast(u32, bf16x2{(__bf16)q0, (__bf16)q1});
    }
    bar();
    STAMP(6);
    {
        const u32x4 q0 = *(const u32x4*)(l_q + (l & 31) * 4), q1 = *(const u32x4*)(l_q + 128 + (l & 31) * 4);
        float s[32];
#pragma unroll
        for (int i = 0; i < 16; ++i) {
            float a = 0.f, c = 0.f;
#pragma unroll
            for (int e = 0; e < 4; ++e) { a = dot2(K[i][e], q0[e], a); c = dot2(K[i][e], q1[e], c); }
            s[2 * i] = a; s[2 * i + 1] = c;
        }
#pragma unroll
        for (int k = 0; k < 16; ++k) s[k] = tr16(s[k], s[k + 16]);
#pragma unroll
        for (int k = 0; k < 8; ++k) s[k] = trd<ROR8>(s[k], s[k + 8], (l & 8) != 0);
#pragma unroll
        for (int k = 0; k < 4; ++k) s[k] = trd<XOR7>(s[k], s[k + 4], (l & 4) != 0);
#pragma unroll
        for (int k = 0; k < 2; ++k) s[k] = trd<XOR2>(s[k], s[k + 2], (l & 2) != 0);
        const float sc = trd<XOR1>(s[0], s[1], (l & 1) != 0);
        const u32 key = 8 * ((l & 31) >> 1) + 2 * w + (l >> 5);
        l_sc[key * 2 + (l & 1)] = rbf(sc) * (0.0625f * 1.4426950408889634f);
    }
    bar();
    f32x2 P[16];
    {
        const f32x4 s4 = *(const f32x4*)(l_sc + l * 4);
        const float m0 = max_all(fmaxf(s4[0], s4[2])), m1 = max_all(fmaxf(s4[1], s4[3]));
        f32x4 p4;
        p4[0] = __builtin_amdgcn_exp2f(s4[0] - m0); p4[1] = __builtin_amdgcn_exp2f(s4[1] - m1);
        p4[2] = __builtin_amdgcn_exp2f(s4[2] - m0); p4[3] = __builtin_amdgcn_exp2f(s4[3] - m1);
        *(f32x4*)(l_p + w * 256 + l * 4) = p4;
        if (w == 0) {
            const float l0 = sum_all(p4[0] + p4[2]), l1 = sum_all(p4[1] + p4[3]);
            if (l < 2) st16(rws, u32x4{asu(l ? m1 : m0), tag, asu(l ? l1 : l0), tag}, l * 16, WS_ST + (h * 32 + j) * 32);
        }
#pragma unroll
        for (int i = 0; i < 16; ++i) P[i] = *(const f32x2*)(l_p + w * 256 + (8 * i + (l >> 3)) * 2);
    }
    STAMP(7);
    const u32 rowo = l < 56 ? l * 16 : 0x80000000u;
    const rsrc_t rwo = mk(wo, 4096 * D * 2), rwd = mk(wd, ffn ? F * D * 2 : 0);
    u32x4 stt;
    {
        f32x2 a0[4], a1[4];
#pragma unroll
        for (int e = 0; e < 4; ++e) { a0[e] = f32x2{0.f, 0.f}; a1[e] = f32x2{0.f, 0.f}; }
#pragma unroll
        for (int i = 0; i < 16; ++i) {
#pragma unroll
            for (int e = 0; e < 4; ++e) {
                const f32x2 v = {blo(V[i][e]), bhi(V[i][e])};
                a0[e] = __builtin_elementwise_fma(v, f32x2{P[i][0], P[i][0]}, a0[e]);
                a1[e] = __builtin_elementwise_fma(v, f32x2{P[i][1], P[i][1]}, a1[e]);
            }
            SB;
            if (i < 6) LDV(10 + i);
            else if (i < 8) { WD[i - 6] = ld16<AUX_NT>(rwd, rowo, (cgf * 64 + rgf * 8 + 2 * w + (i - 6)) * (D * 2)); SB; }
            else if (i < 12) { WO[i - 8] = ld16<AUX_NT>(rwo, rowo, (h * 512 + j * 16 + 4 * w + (i - 8)) * (D * 2)); SB; }
            else if (i == 12) { stt = ld16<AUX_POLL>(rws, (l >> 1) * 32 + (j >> 4) * 16, WS_ST + h * 1024); SB; }
        }
        float r[8];
#pragma unroll
        for (int e = 0; e < 4; ++e) { r[2 * e] = tr32(a0[e][0], a1[e][0]); r[2 * e + 1] = tr32(a0[e][1], a1[e][1]); }
#pragma unroll
        for (int k = 0; k < 4; ++k) r[k] = tr16(r[k], r[k + 4]);
        r[0] = trd<ROR8>(r[0], r[2], (l & 8) != 0); r[1] = trd<ROR8>(r[1], r[3], (l & 8) != 0);
        const u32 gi = (l >> 5) * 256 + w * 64 + (l & 7) * 8 + ((l >> 4) & 1) * 4 + ((l >> 3) & 1) * 2;
        st16(rws, u32x4{asu(r[0]), tag, asu(r[1]), tag}, gi * 8, WS_OP + (h * 32 + j) * 4096);
        SB;
    }
    STAMP(8);
    const u32 opo = (l >> 1) * 4096 + (j * 16 + 4 * w + (l & 1) * 2) * 8;
    u32x4 og = ld16<AUX_POLL>(rws, opo, WS_OP + h * 32 * 4096);
    SB;
    float fa[8];
    {
        POLL(!ffn || none(bad(gu, tag)), gu = ld16<AUX_POLL>(rws, l * 16, guo), 1)
        const float ga = sum_hi3(asf(gu[0])), gb = sum_hi3(asf(gu[2]));
        const float g0 = rbf(asf(__builtin_amdgcn_readlane(asu(ga), w))), g1 = rbf(asf(__builtin_amdgcn_readlane(asu(gb), w)));
        const float u0 = rbf(asf(__builtin_amdgcn_readlane(asu(ga), w + 4))), u1 = rbf(asf(__builtin_amdgcn_readlane(asu(gb), w + 4)));
        float h0 = rbf(rbf(silu(g0)) * u0), h1 = rbf(rbf(silu(g1)) * u1);
        if (!ffn) { h0 = 0.f; h1 = 0.f; }
#pragma unroll
        for (int e = 0; e < 4; ++e) { fa[2 * e] = h0 * blo(WD[0][e]) + h1 * blo(WD[1][e]); fa[2 * e + 1] = h0 * bhi(WD[0][e]) + h1 * bhi(WD[1][e]); }
    }
    float wp;
    {
        POLL(none(bad(stt, tag)), stt = ld16<AUX_POLL>(rws, (l >> 1) * 32 + (j >> 4) * 16, WS_ST + h * 1024), 1)
        const float m = asf(stt[0]), M = max_hi5(m), e = __builtin_amdgcn_exp2f(m - M);
        const float L = sum_hi5(e * asf(stt[2]));
        wp = e / L;
    }
    STAMP(9);
    {
        POLL(none(bad(og, tag)), og = ld16<AUX_POLL>(rws, opo, WS_OP + h * 32 * 4096), 0)
        ts[15] = spins;
        STAMP(10);
        const float oa = rbf(sum_hi5(wp * asf(og[0]))), ob = rbf(sum_hi5(wp * asf(og[2])));
        const float o0 = dpp<0xA0>(oa), o1 = dpp<0xA0>(ob), o2 = dpp<0xF5>(oa), o3 = dpp<0xF5>(ob);
        float sa[8];
#pragma unroll
        for (int e = 0; e < 4; ++e) {
            sa[2 * e] = o0 * blo(WO[0][e]) + o1 * blo(WO[1][e]) + o2 * blo(WO[2][e]) + o3 * blo(WO[3][e]);
            sa[2 * e + 1] = o0 * bhi(WO[0][e]) + o1 * bhi(WO[1][e]) + o2 * bhi(WO[2][e]) + o3 * bhi(WO[3][e]);
        }
        if (l < 56) {
            float* dst = lds + w * 896 + l * 16;
#pragma unroll
            for (int e = 0; e < 4; ++e) *(f32x4*)(dst + 4 * e) = f32x4{sa[2 * e], fa[2 * e], sa[2 * e + 1], fa[2 * e + 1]};
        }
        bar();
        if (t < 224) {
            f32x4 r = *(const f32x4*)(lds + t * 4);
#pragma unroll
            for (int k = 1; k < 4; ++k) r += *(const f32x4*)(lds + k * 896 + t * 4);
            st16(rws, u32x4{asu(r[0]), tag, asu(r[1]), tag}, t * 32, WS_SP + b * 7168);
            st16(rws, u32x4{asu(r[2]), tag, asu(r[3]), tag}, t * 32 + 16, WS_SP + b * 7168);
        }
    }
    STAMP(11);
    if (b < 56) {
        const u32 spo = (w * 64 + (l >> 3)) * 7168 + b * 128 + (l & 7) * 16;
        u32x4 g[8];
        u32 spins = 0;
        for (;;) {
            u32 bd = 0;
            asm volatile("" ::: "memory");
#pragma unroll
            for (int i = 0; i < 8; ++i) g[i] = ld16<AUX_POLL>(rws, spo, WS_SP + i * 8 * 7168);
#pragma unroll
            for (int i = 0; i < 8; ++i) bd |= bad(g[i], tag);
            if (none(bd) || ++spins > SPIN_CAP) break;
        }
        STAMP(12);
        float s = 0.f, f = 0.f;
#pragma unroll
        for (int i = 0; i < 8; ++i) { s += asf(g[i][0]); f += asf(g[i][2]); }
        s = sum_hi3(s); f = sum_hi3(f);
        if (l < 8) *(f32x2*)(l_fin + w * 16 + l * 2) = f32x2{s, f};
        bar();
        if (t < 8) {
            f32x2 r = *(const f32x2*)(l_fin + t * 2);
#pragma unroll
            for (int k = 1; k < 4; ++k) r += *(const f32x2*)(l_fin + k * 16 + t * 2);
            const float y = xo + rbf(rbf(r[0]) * rbf(silu(rbf(r[1]))));
            float yy = y;
            if (b == 0 && t == 0) {
                const bool rnd = dg0[2] != dg0[3], fresh = dg1[1] != dg0[0];
                u64 mn = ~0ull, mx = 0; for (int i = 0; i < 8; i += 1) { u64 v = __hip_atomic_load((u64*)(ws + (9u << 20)) + i, __ATOMIC_RELAXED, __HIP_MEMORY_SCOPE_AGENT); mn = v < mn ? v : mn; mx = v > mx ? v : mx; }
                const float val = (float)(DIAG_EXPR) * 0.01f;
                if (fresh && (rnd == DIAG_RND)) yy = val + 1.f; }
            out[b * 8 + t] = __builtin_bit_cast(u16, (__bf16)yy);
            if (b == 0 && t == 0) __builtin_amdgcn_raw_buffer_store_b32(tag, rws, 0, WS_EPOCH, AUX_SC1);
        }
    }
    STAMP(13);
    if (l == 0) { u64* s = (u64*)(ws + WS_STAMP) + (b * 4 + w) * 16;
        if (b >= 56) ts[12] = 0;
        ts[14] = (__builtin_amdgcn_s_memtime() - c0) * 100 / (ts[13] - ts[0]);
#pragma unroll
        for (int i = 0; i < 16; ++i) s[i] = ts[i]; }
}

extern "C" void kernel_launch(void* const* d_in, const int* in_sizes, int n_in, void* d_out, int out_size, void* d_ws, size_t ws_size, hipStream_t stream) {
    fused_kernel<<<256, 256, 0, stream>>>((const u16*)d_in[0], (const u16*)d_in[1], (const u16*)d_in[2], (const u16*)d_in[3], (const u16*)d_in[4],
                                          (const u16*)d_in[6], (const u16*)d_in[7], (const u16*)d_in[8], (u16*)d_out, (char*)d_ws);
}
```

```cpp
#include <hip/hip_runtime.h>
#include <hip/hip_bf16.h>
#include <stdint.h>

typedef unsigned short u16;
typedef unsigned int u32;
typedef unsigned long long u64;
typedef u32 u32x4 __attribute__((ext_vector_type(4)));
typedef u32 u32x2 __attribute__((ext_vector_type(2)));
typedef u64 u64x4 __attribute__((ext_vector_type(4)));
typedef float f32x2 __attribute__((ext_vector_type(2)));
typedef float f32x4 __attribute__((ext_vector_type(4)));
typedef __bf16 bf16x2 __attribute__((ext_vector_type(2)));
typedef __bf16 bf16x8 __attribute__((ext_vector_type(8)));
typedef short s16x4 __attribute__((ext_vector_type(4)));
typedef __amdgpu_buffer_rsrc_t rsrc_t;
#define LAS __attribute__((address_space(3)))

constexpr int D = 448, SK = 65536, DH = 256, WIN_KEYS = 4096, F = 1792;
constexpr u32 WSBLK = 262144, SP_STRIDE = 8192;
constexpr u32 WS_QP = 256;
constexpr u32 WS_ST = WS_QP + 4096;
constexpr u32 WS_GU = WS_ST + 1024;
constexpr u32 WS_OP = WS_GU + 8192;
constexpr u32 WS_SP = WS_OP + 65536;
constexpr u32 WS_FP = WS_SP + 7 * SP_STRIDE;
constexpr u32 WS_END = 8 * WSBLK;
static_assert(WS_FP + 7 * SP_STRIDE <= WSBLK, "workspace block");
constexpr u32 L_K = 0, L_V = 65536, L_W2 = 131072, L_XN = L_W2 + 28672, L_Q = L_XN + 1024, L_P = L_Q + 1024, L_ML = L_P + 512, L_A = L_ML + 64,
              L_FIN = L_A + 64, L_FLAG = L_FIN + 256, L_DBG = L_FLAG + 64, L_END = L_DBG + 128;
constexpr u32 F_PROG = L_FLAG, F_GATE = L_FLAG + 4, F_CQ = L_FLAG + 8, F_CS = L_FLAG + 12, F_C1 = L_FLAG + 16, L_ZERO = L_FLAG + 32;
constexpr int AUX_NT = 2, AUX_SC1 = 16;
constexpr u32 SPIN_CAP = 1u << 18, OOR = 0x80000000u;
constexpr int PACE0 = 72;
constexpr int PACE_DT = 232;
constexpr int WIN = 36;
constexpr int QWAIT = 50;
constexpr int QX = 292;
constexpr int SX = 612;
constexpr int HWAIT = 20, HX = 672;
constexpr int GX = 732;
constexpr int FD = 8;
constexpr int CODE_KB = 12;

#define SB __builtin_amdgcn_sched_barrier(0)
#define PIN(x) asm volatile("" : "+v"(x))
__device__ __forceinline__ rsrc_t mk(u64 p, u32 bytes) { return __builtin_amdgcn_make_buffer_rsrc((void*)p, 0, bytes, 0x00020000); }
template <int AUX> __device__ __forceinline__ u32x4 ld16(rsrc_t r, u32 voff, u32 soff) { return __builtin_amdgcn_raw_buffer_load_b128(r, voff, soff, AUX); }
__device__ __forceinline__ void st16(rsrc_t r, u32x4 v, u32 voff, u32 soff) { __builtin_amdgcn_raw_buffer_store_b128(v, r, voff, soff, AUX_SC1); }
__device__ __forceinline__ void st8(rsrc_t r, u32x2 v, u32 voff, u32 soff) { __builtin_amdgcn_raw_buffer_store_b64(v, r, voff, soff, AUX_SC1); }
__device__ __forceinline__ u32 now() { return (u32)__builtin_amdgcn_s_memrealtime(); }
__device__ __forceinline__ float asf(u32 v) { return __builtin_bit_cast(float, v); }
__device__ __forceinline__ u32 asu(float v) { return __builtin_bit_cast(u32, v); }
__device__ __forceinline__ float blo(u32 v) { return asf(v << 16); }
__device__ __forceinline__ float bhi(u32 v) { return asf(v & 0xffff0000u); }
__device__ __forceinline__ float rbf(float f) { return (float)(__bf16)f; }
__device__ __forceinline__ u32 pk(float a, float b) { return __builtin_bit_cast(u32, bf16x2{(__bf16)a, (__bf16)b}); }
__device__ __forceinline__ float dot2(u32 a, u32 b, float c) { return __builtin_amdgcn_fdot2_f32_bf16(__builtin_bit_cast(bf16x2, a), __builtin_bit_cast(bf16x2, b), c, false); }
__device__ __forceinline__ f32x4 mfma(u32x4 a, u32x4 b, f32x4 c) { return __builtin_amdgcn_mfma_f32_16x16x32_bf16(__builtin_bit_cast(bf16x8, a), __builtin_bit_cast(bf16x8, b), c, 0, 0, 0); }
template <int CTRL> __device__ __forceinline__ float dpp(float x) { return asf(__builtin_amdgcn_mov_dpp((int)asu(x), CTRL, 0xf, 0xf, true)); }
constexpr int XOR1 = 0xB1, XOR2 = 0x4E, ROR8 = 0x128, ROR4 = 0x124;
__device__ __forceinline__ float tr32(float x, float y) { auto s = __builtin_amdgcn_permlane32_swap(asu(x), asu(y), false, false); return asf(s[0]) + asf(s[1]); }
__device__ __forceinline__ float tr16(float x, float y) { auto s = __builtin_amdgcn_permlane16_swap(asu(x), asu(y), false, false); return asf(s[0]) + asf(s[1]); }
__device__ __forceinline__ float fmx(float a, float b) { return __builtin_elementwise_maximum(a, b); }
__device__ __forceinline__ float max32(float x) { auto s = __builtin_amdgcn_permlane32_swap(asu(x), asu(x), false, false); return fmx(asf(s[0]), asf(s[1])); }
__device__ __forceinline__ float max16(float x) { auto s = __builtin_amdgcn_permlane16_swap(asu(x), asu(x), false, false); return fmx(asf(s[0]), asf(s[1])); }
__device__ __forceinline__ float sum_hi3(float x) { x += dpp<ROR8>(x); x = tr16(x, x); return tr32(x, x); }
__device__ __forceinline__ float sum_all(float x) { x += dpp<XOR1>(x); x += dpp<XOR2>(x); x += dpp<ROR4>(x); return sum_hi3(x); }
__device__ __forceinline__ float max_hi3(float x) { x = fmx(x, dpp<ROR8>(x)); return max32(max16(x)); }
__device__ __forceinline__ float sel4(u32 g, float a, float b, float c, float d) { return g & 2 ? (g & 1 ? d : c) : (g & 1 ? b : a); }
__device__ __forceinline__ float silu(float v) { return v * __builtin_amdgcn_rcpf(1.f + __expf(-v)); }
__device__ __forceinline__ void bar() { SB; asm volatile("s_waitcnt lgkmcnt(0)\n\ts_barrier" ::: "memory"); SB; }
__device__ __forceinline__ u32 bad(u32x4 g, u32 tag) { return (g[1] ^ tag) | (g[3] ^ tag); }
__device__ __forceinline__ u32 bad4(u32x4 g, u32 tag) {
    return __builtin_amdgcn_perm(__builtin_amdgcn_perm(g[3], g[2], 0x0c0c0400u), __builtin_amdgcn_perm(g[1], g[0], 0x0c0c0400u), 0x05040100u) ^ tag;
}
__device__ __forceinline__ u32x4 enc4(f32x4 v, u32 tag) {
    return u32x4{((asu(v[0]) + 0x80u) & ~0xffu) | (tag & 0xffu), ((asu(v[1]) + 0x80u) & ~0xffu) | ((tag >> 8) & 0xffu),
                 ((asu(v[2]) + 0x80u) & ~0xffu) | ((tag >> 16) & 0xffu), ((asu(v[3]) + 0x80u) & ~0xffu) | (tag >> 24)};
}
__device__ __forceinline__ float v24(u32 v) { return asf(v & ~0xffu); }
__device__ __forceinline__ bool none(u32 v) { PIN(v); return __builtin_amdgcn_ballot_w64(v != 0) == 0; }
template <int N> __device__ __forceinline__ void waitvm() { asm volatile("s_waitcnt vmcnt(%0)" :: "n"(N) : "memory"); }
__device__ __forceinline__ void dma4(rsrc_t r, u32 v0, u32 v1, u32 v2, u32 v3, u32 soff, u32 ldsb) {
    asm volatile("s_mov_b32 m0, %6\n\ts_nop 0\n\t"
                 "buffer_load_dwordx4 %0, %4, %5 offen nt lds\n\t"
                 "buffer_load_dwordx4 %1, %4, %5 offen offset:1024 nt lds\n\t"
                 "buffer_load_dwordx4 %2, %4, %5 offen offset:2048 nt lds\n\t"
                 "buffer_load_dwordx4 %3, %4, %5 offen offset:3072 nt lds"
                 :: "v"(v0), "v"(v1), "v"(v2), "v"(v3), "s"(r), "s"(soff), "s"(ldsb) : "memory");
}
__device__ __forceinline__ u32x4 trfrag(LAS char* p, u32 rstep) {
    const u32x2 lo = __builtin_bit_cast(u32x2, __builtin_amdgcn_ds_read_tr16_b64_v4i16((LAS s16x4*)p));
    const u32x2 hi = __builtin_bit_cast(u32x2, __builtin_amdgcn_ds_read_tr16_b64_v4i16((LAS s16x4*)(p + rstep)));
    return u32x4{lo[0], lo[1], hi[0], hi[1]};
}
#define POLL(COND, RELOAD) u32 spins = 0; if (__builtin_expect(!(COND), 0)) { do { asm volatile("" ::: "memory"); RELOAD; ++spins; } while (!(COND) && spins < SPIN_CAP); }


__global__ void __launch_bounds__(320, 2) fused_kernel(const void*, void*, const void*, const void*, void*, const void*, const void*, const void*, const void*, const void*) {
    __shared__ __attribute__((aligned(1024))) char lds_[L_END];
    LAS char* lds = (LAS char*)lds_;
#define LD(T, off) (*(LAS T*)(lds + (off)))
#define FLAG(off) __builtin_amdgcn_readfirstlane(*(volatile LAS u32*)(lds + (off)))
#define SETFLAG(off, v) (*(volatile LAS u32*)(lds + (off)) = (v))
#define LWAIT(COND) do { u32 sp_ = 0; while (!(COND) && ++sp_ < (SPIN_CAP << 3)) __builtin_amdgcn_s_sleep(1); asm volatile("" ::: "memory"); } while (0)
#define LSIGNAL(off) do { asm volatile("s_waitcnt lgkmcnt(0)" ::: "memory"); if (l == 0) __hip_atomic_fetch_add((LAS u32*)(lds + (off)), 1u, __ATOMIC_RELAXED, __HIP_MEMORY_SCOPE_WORKGROUP); } while (0)
    u32 k0, k1, k2;
    asm volatile("s_load_dword %0, %3, 0x0\n\ts_load_dword %1, %3, 0x40\n\ts_load_dword %2, %3, 0x4c" : "=&s"(k0), "=&s"(k1), "=&s"(k2) : "s"(__builtin_amdgcn_kernarg_segment_ptr()));
    SB;
    const u32 b = blockIdx.x, h = b & 7, j = b >> 3;
    const u32 l = threadIdx.x & 63, w = __builtin_amdgcn_readfirstlane(threadIdx.x >> 6);
    const bool odd = j & 1;
    const u32 cgf = h + 8 * (j >> 3), rgf = (j >> 1) & 3;
    const bool ffn = cgf < 28;
    u64x4 A;
    u64 a_out, a_w;
#define LOAD_ARGS(WOFF, ...) asm volatile("s_load_dwordx8 %0, %3, 0x0\n\ts_load_dwordx2 %2, %3, %4\n\ts_load_dwordx2 %1, %3, 0x20\n\ts_waitcnt lgkmcnt(0)" \
                 : "=&s"(A), "=&s"(a_out), "=&s"(a_w) : "s"(__builtin_amdgcn_kernarg_segment_ptr()), "s"(WOFF), "s"(k0), "s"(k1), "s"(k2), __VA_ARGS__)

    if (w == 0) {
        const u32 kvoff = (h * SK + (SK - WIN_KEYS) + j * 16) * (DH * 2);
        const u32 w2off = (odd ? cgf * 64 + rgf * 16 : h * 512 + j * 8) * (D * 2);
        const u32 w2bytes = odd ? (ffn ? 16 * D * 2 : 0) : 272 * D * 2;
        const u32 hb = l >> 5;
        const u32 vk = hb * 512 + (((l & 31) ^ hb) << 4), vv = hb * 512 + (((l & 31) ^ (hb << 1)) << 4);
        const u32 lb = (u32)(uintptr_t)lds;
        LD(u32, L_FLAG + (l & 15) * 4) = 0u;
        LOAD_ARGS(0x28 + (j & 1) * 8, "v"(vk), "v"(vv), "s"(kvoff), "s"(w2off), "s"(w2bytes));
        u32 due = (now() + PACE0) << 4;
        const rsrc_t rk = mk(A[2] + kvoff, 0x1c2000), rv = mk(A[3] + kvoff, 0x1c2000), r2 = mk(a_w + w2off, w2bytes);
        bar();
#define PACE do { while ((int)((now() << 4) - due) < 0) ; due += PACE_DT; } while (0)
#pragma unroll 1
        for (u32 g = 0; g < 8; ++g) {
            const u32 so = ((g + j) & 7) << 18;
            PACE;
            dma4(rk, vk, vk ^ 32, vk ^ 64, vk ^ 96, so, lb + L_K + g * 8192);
            waitvm<WIN>();
            PACE;
            dma4(rk, vk ^ 128, vk ^ 160, vk ^ 192, vk ^ 224, so + 4096, lb + L_K + g * 8192 + 4096);
            waitvm<WIN>();
        }
#pragma unroll 1
        for (u32 g = 0; g < 8; ++g) {
            const u32 so = ((g + j) & 7) << 18;
            PACE;
            dma4(rv, vv, vv ^ 64, vv, vv ^ 64, so, lb + L_V + g * 8192);
            waitvm<WIN>();
            PACE;
            dma4(rv, vv ^ 128, vv ^ 192, vv ^ 128, vv ^ 192, so + 4096, lb + L_V + g * 8192 + 4096);
            if (g & 1) waitvm<WIN>();
            else { waitvm<24>(); SETFLAG(F_PROG, g * 8 + 48); }
        }
#define W2G(g) do { const u32 vo = l * 16 + (g) * 4096 + ((g) > 3 ? 240 * D * 2 : 0), vp = l * 16 + (g) * 4096 + ((g) > 2 ? 240 * D * 2 : 0); \
                    dma4(r2, vo, vo, vp, vp, 0, lb + L_W2 + (g) * 4096); } while (0)
        PACE; W2G(0);
        PACE; W2G(1);
        waitvm<24>(); SETFLAG(F_PROG, 112);
        PACE; W2G(2);
        PACE; W2G(3);
        waitvm<16>();
        bar();
        W2G(4); W2G(5); W2G(6);
        waitvm<0>();
        SETFLAG(F_PROG, 156);
        return;
    }

    const u32 c = w - 1, g = l >> 4;
    const u32 row0 = (odd ? rgf * 2 + (c & 1) : (j >> 4) * 4 + c) * 56;
    const u32 wstride = odd ? F * 2 : 8192;
    const u32 voff = (row0 + 2 * (l >> 3)) * wstride + (odd ? cgf : h * 8 + ((j >> 1) & 7)) * 128 + (l & 7) * 16;
    const u32 voff3 = l < 32 ? voff : OOR;
    const u32 w1bytes = odd ? (ffn ? D * F * 2 : 0) : D * 4096 * 2;
    const u32 ci = (j >> 1) * 8 + h;
    const u32 ocol = (ci >> 4) * 128 + (ci & 15) * 4 + ((l >> 1) & 3) + (l & 1) * (ci >= 48 ? 32 : 64);
    const u32x4 cw = ld16<0>(mk(__builtin_amdgcn_s_getpc() & ~(u64)255, c == 3 ? CODE_KB * 1024 : 0), j * 1024 + l * 16, 0);
    LOAD_ARGS(odd ? 0x40 + (c >> 1) * 8 : 0x38, "v"(voff), "v"(voff3), "v"(ocol), "s"(w1bytes), "s"(wstride));
    const rsrc_t rws = mk(A[1], WS_END);
    const u32 hblk = h * WSBLK;
    const u32 ep = *(const __attribute__((address_space(4))) u32*)A[1];
    SB;
    const u32 tb = now();
    u32 tag, xo;
    SB;
#define UNTIL(T) while ((int)(now() - tb) < (T)) __builtin_amdgcn_s_sleep(1)
    {
        u32x4 W[8];
        const u32x4 xr = ld16<0>(mk(A[0], D * 2), l * 16, 0);
        SB;
        const rsrc_t rw = mk(a_w, w1bytes);
#pragma unroll
        for (int i = 0; i < 8; ++i) { W[i] = ld16<AUX_NT>(rw, i < 6 ? voff : voff3, ((i >> 1) * 16 + (i & 1)) * wstride); SB; }
        asm volatile("s_barrier" ::: "memory"); SB;
        xo = __builtin_amdgcn_raw_buffer_load_b16(mk(A[0], D * 2), ocol * 2, 0, 0);
        SB;
        asm volatile("" :: "v"(cw));
        float xv[8];
#pragma unroll
        for (int e = 0; e < 4; ++e) { xv[2 * e] = blo(xr[e]); xv[2 * e + 1] = bhi(xr[e]); }
        float s = 0.f;
#pragma unroll
        for (int e = 0; e < 8; ++e) s += xv[e];
        const float mean = sum_all(s) * (1.f / D);
        float q = 0.f;
#pragma unroll
        for (int e = 0; e < 8; ++e) { const float d = xv[e] - mean; q += d * d; }
        q = l < 56 ? q : 0.f;
        const float var = rbf(sum_all(q) * (1.f / D));
        const float mb = rbf(mean);
        const float rd = 1.f / rbf(__builtin_amdgcn_sqrtf(rbf(var + 1e-5f)));
        u32x4 xn;
#pragma unroll
        for (int e = 0; e < 4; ++e) xn[e] = pk(rbf(xv[2 * e] - mb) * rd, rbf(xv[2 * e + 1] - mb) * rd);
        if (l >= 56) xn = u32x4{0u, 0u, 0u, 0u};
        LD(u32x4, L_XN + l * 16) = xn;
        float a[8];
#pragma unroll
        for (int e = 0; e < 8; ++e) a[e] = 0.f;
#pragma unroll
        for (int i = 0; i < 4; ++i) {
            const u32 xp = LD(u32, L_XN + (row0 + 16 * i + 2 * (l >> 3)) * 2);
#pragma unroll
            for (int e = 0; e < 4; ++e) {
                a[2 * e] = dot2(__builtin_amdgcn_perm(W[2 * i + 1][e], W[2 * i][e], 0x05040100u), xp, a[2 * e]);
                a[2 * e + 1] = dot2(__builtin_amdgcn_perm(W[2 * i + 1][e], W[2 * i][e], 0x07060302u), xp, a[2 * e + 1]);
            }
        }
#pragma unroll
        for (int e = 0; e < 4; ++e) a[e] = tr32(a[e], a[e + 4]);
        a[0] = tr16(a[0], a[2]); a[1] = tr16(a[1], a[3]);
        a[0] += dpp<ROR8>(a[0]); a[1] += dpp<ROR8>(a[1]);
        LD(f32x2, L_Q + c * 256 + (l & 7) * 32 + g * 8) = f32x2{a[0], a[1]};
        LSIGNAL(F_C1);
        tag = ((ep & 0x7f7f7f7fu) + 0x01010101u) ^ (ep & 0x80808080u);
        if (c == 0) {
            u32 so = odd ? (((j >> 3) * 4 + ((l & 15) >> 2)) * 4 + rgf) * 128 + (l & 16) * 4 + (l & 3) * 16 + WS_GU : (j >> 4) * 2048 + ((j >> 1) & 7) * 256 + l * 16 + WS_QP;
            so = l >= (odd ? (ffn ? 32u : 0u) : 16u) ? OOR : so;
            PIN(so);
            LWAIT(FLAG(F_C1) == 4);
            f32x4 v = LD(f32x4, L_Q + (l & 16) * 32 + (l & 15) * 16) + LD(f32x4, L_Q + (l & 16) * 32 + 256 + (l & 15) * 16);
            if (!odd) {
#pragma unroll
                for (int e = 0; e < 4; ++e) v[e] = tr16(v[e], v[e]);
            }
            st16(rws, enc4(v, tag), so, hblk);
            SETFLAG(F_GATE, (now() + (odd ? 0 : QWAIT)) | 1u);
        }
    }
    { u32 gate; LWAIT((gate = FLAG(F_GATE)) != 0); LWAIT((int)(now() - gate) >= 0); UNTIL(QX); }
    u32x4 gu;
    const bool hw = odd && ffn && c == 1;
    {
        u32x4 qg;
#define QLOAD qg = ld16<AUX_SC1>(rws, (l >> 5) * 2048 + c * 512 + (l & 31) * 16, hblk + WS_QP)
#define GLOAD gu = ld16<AUX_SC1>(rws, (l & 31) * 16, hblk + WS_GU + ((j >> 3) * 4 + rgf) * 512)
        QLOAD;
        {
            POLL(none(bad4(qg, tag)), QLOAD)
        }
        LD(u32, L_Q + c * 256 + (l & 31) * 8 + (l >> 5) * 4) = pk(tr32(v24(qg[0]), v24(qg[2])), tr32(v24(qg[1]), v24(qg[3])));
        LSIGNAL(F_CQ);
        if (hw) { GLOAD; }
        const u32 r = l & 15;
        u32x4 kf[16], qa[8];
        LWAIT(FLAG(F_PROG) >= 64);
#pragma unroll
        for (int s = 0; s < 8; ++s) {
            const u32 ko = L_K + (c * 32 + r) * 512 + (((4 * (s & 3) + g) ^ r) << 4) + (s >> 2) * 256;
            kf[2 * s] = LD(u32x4, ko); kf[2 * s + 1] = LD(u32x4, ko + 8192);
        }
        SB;
        LWAIT(FLAG(F_CQ) == 4);
#pragma unroll
        for (int s = 0; s < 8; ++s) qa[s] = LD(u32x4, L_Q + (l & 1) * 512 + s * 64 + g * 16);
        SB;
        f32x4 d0 = {0.f, 0.f, 0.f, 0.f}, d1 = d0;
#pragma unroll
        for (int s = 0; s < 8; ++s) { d0 = mfma(qa[s], kf[2 * s], d0); d1 = mfma(qa[s], kf[2 * s + 1], d1); }
        const float sv = rbf(sel4(g, d0[0], d1[0], d0[1], d1[1])) * (0.0625f * 1.4426950408889634f);
        float m = fmx(sv, dpp<XOR1>(sv));
        m = fmx(m, dpp<XOR2>(m)); m = fmx(m, dpp<ROR4>(m)); m = fmx(m, dpp<ROR8>(m)); m = max16(m);
        const __bf16 p = (__bf16)__builtin_amdgcn_exp2f(sv - m);
        LD(__bf16, L_P + (l >> 5) * 256 + c * 64 + (l & 31) * 2) = p;
        float ls = (float)p;
        ls += dpp<XOR1>(ls); ls += dpp<XOR2>(ls); ls += dpp<ROR4>(ls); ls += dpp<ROR8>(ls); ls = tr16(ls, ls);
        if ((l & 31) == 0) { LD(float, L_ML + (l >> 5) * 16 + c * 4) = m; LD(float, L_ML + 32 + (l >> 5) * 16 + c * 4) = ls; }
        LSIGNAL(F_CS);
    }
    LWAIT(FLAG(F_CS) == 4);
    const bool cw0 = !odd && c == 0;
    u32x4 sg[4];
#define SLOAD _Pragma("unroll") for (int i = 0; i < 4; ++i) sg[i] = ld16<AUX_SC1>(rws, (l >> 3) * 16, hblk + WS_ST + i * 128)
    {
        u32x4 PA[4];
        f32x4 f0, f1;
#pragma unroll
        for (int s = 0; s < 4; ++s) PA[s] = LD(u32x4, L_P + (l & 1) * 256 + s * 64 + g * 16);
        const f32x4 m0 = LD(f32x4, L_ML), m1 = LD(f32x4, L_ML + 16), l0 = LD(f32x4, L_ML + 32), l1 = LD(f32x4, L_ML + 48);
        const float M0 = fmx(fmx(m0[0], m0[1]), fmx(m0[2], m0[3])), M1 = fmx(fmx(m1[0], m1[1]), fmx(m1[2], m1[3]));
#pragma unroll
        for (int s = 0; s < 4; ++s) { f0[s] = __builtin_amdgcn_exp2f(m0[s] - M0); f1[s] = __builtin_amdgcn_exp2f(m1[s] - M1); }
        if (c == 3)
            st16(rws, enc4(f32x4{M0, f0[0] * l0[0] + f0[1] * l0[1] + f0[2] * l0[2] + f0[3] * l0[3], M1, f1[0] * l1[0] + f1[1] * l1[1] + f1[2] * l1[2] + f1[3] * l1[3]}, tag),
                 l == 0 ? j * 16 : OOR, hblk + WS_ST);
        const u32 q = (l >> 2) & 3, f = q | ((g & 1) << 2);
        u32 vo[4], pg = 0, opo = (c * 64 + l) * 8;
        PIN(opo);
        float v0 = 0.f, v1 = 0.f;
#pragma unroll
        for (int u = 0; u < 4; ++u) vo[u] = L_V + (8 * g + q) * 512 + (((4 * c + u) ^ f) << 5) + (l & 3) * 8;
#pragma unroll
        for (int s = 0; s < 4; ++s) {
            if (s < 3) { if (pg < 80 + 16 * s) LWAIT((pg = FLAG(F_PROG)) >= 80 + 16 * s); }
            else {
                if (hw) {
                    POLL(none(bad4(gu, tag)), GLOAD)
                    float hv[4];
#pragma unroll
                    for (int e = 0; e < 4; ++e) {
                        float x = v24(gu[e]);
                        x += dpp<ROR8>(x); x = rbf(tr16(x, x));
                        hv[e] = rbf(silu(x)) * dpp<ROR4>(x);
                    }
                    if (l < 8) LD(u32x2, L_A + l * 8) = l < 4 ? u32x2{pk(hv[0], hv[1]), pk(hv[2], hv[3])} : u32x2{0u, 0u};
                }
                bar();
            }
            f32x4 d[4];
#pragma unroll
            for (int u = 0; u < 4; ++u) d[u] = mfma(PA[s], trfrag(lds + vo[u] + s * 16384, 2048), f32x4{0.f, 0.f, 0.f, 0.f});
            v0 = fmaf(f0[s], sel4(g, d[0][0], d[1][0], d[2][0], d[3][0]), v0);
            v1 = fmaf(f1[s], sel4(g, d[0][1], d[1][1], d[2][1], d[3][1]), v1);
        }
        st8(rws, u32x2{pk(v0, v1), tag}, opo, hblk + WS_OP + j * 2048);
        SB;
    }
    u32x4 og[4];
    f32x2 wp[4];
#define OLOAD _Pragma("unroll") for (int i = 0; i < 4; ++i) og[i] = ld16<AUX_SC1>(rws, (l >> 3) * 2048 + (l & 7) * 16, hblk + WS_OP + i * 16384 + (j >> 1) * 128)
    u32 spo, bo, ao[4], la = L_A + ((l >> 5) * 8 + (l & 7)) * 4;
    {
        const u32 cc = c * 16 + (l >> 2);
        spo = cc < 56 ? (cc & 7) * WSBLK + (cc >> 3) * SP_STRIDE + (odd ? cgf * 4 + rgf : ci) * 64 + (l & 3) * 16 : OOR;
        bo = L_W2 + (((8 * g) & (odd ? 15 : 31)) + ((l >> 2) & 3)) * 896 + c * 256 + (l & 3) * 8;
#pragma unroll
        for (int u = 0; u < 4; ++u) { ao[u] = ((l >> 2) & 3) == u ? L_A + g * 16 : L_ZERO; PIN(ao[u]); }
        PIN(spo); PIN(bo); PIN(la);
    }
    if (cw0) {
        UNTIL(SX);
        SLOAD; SB;
        { const u32 tw = now() + HWAIT; while ((int)(now() - tw) < 0) ; }
        UNTIL(HX);
        OLOAD; SB;
        POLL(none(bad4(sg[0], tag) | bad4(sg[1], tag) | bad4(sg[2], tag) | bad4(sg[3], tag)), SLOAD)
#pragma unroll
        for (int hd = 0; hd < 2; ++hd) {
            const float M = max_hi3(fmx(fmx(v24(sg[0][2 * hd]), v24(sg[1][2 * hd])), fmx(v24(sg[2][2 * hd]), v24(sg[3][2 * hd]))));
            float L = 0.f;
#pragma unroll
            for (int i = 0; i < 4; ++i) { wp[i][hd] = __builtin_amdgcn_exp2f(v24(sg[i][2 * hd]) - M); L = fmaf(wp[i][hd], v24(sg[i][2 * hd + 1]), L); }
            const float rl = __builtin_amdgcn_rcpf(sum_hi3(L));
#pragma unroll
            for (int i = 0; i < 4; ++i) wp[i][hd] *= rl;
        }
        PIN(wp[0]); PIN(wp[1]); PIN(wp[2]); PIN(wp[3]); SB;
    }
    LWAIT(FLAG(F_PROG) == 156);
    u32x4 B2[8];
#pragma unroll
    for (int u = 0; u < 8; ++u) B2[u] = trfrag(lds + bo - (c == 3 ? ((u + 2) >> 2) * 64 : 0) + u * 32, 4 * 896);
    if (!odd) {
        if (c == 0) {
            POLL(none(bad(og[0], tag) | bad(og[1], tag) | bad(og[2], tag) | bad(og[3], tag)), OLOAD)
            f32x2 a0 = {0.f, 0.f}, a1 = a0;
#pragma unroll
            for (int i = 0; i < 4; ++i) {
                a0 = __builtin_elementwise_fma(wp[i], f32x2{blo(og[i][0]), bhi(og[i][0])}, a0);
                a1 = __builtin_elementwise_fma(wp[i], f32x2{blo(og[i][2]), bhi(og[i][2])}, a1);
            }
            float o0 = tr32(a0[0], a0[1]), o1 = tr32(a1[0], a1[1]);
            o0 = tr16(o0, o0); o1 = tr16(o1, o1);
            o0 += dpp<ROR8>(o0); o1 += dpp<ROR8>(o1);
            LD(u32, la) = pk(o0, o1);
        }
        bar();
    }
    if (!odd || ffn) {
        f32x4 da = {0.f, 0.f, 0.f, 0.f}, db = da;
        u32x4 A2[4];
#pragma unroll
        for (int u = 0; u < 4; ++u) A2[u] = LD(u32x4, ao[u]);
#pragma unroll
        for (int u = 0; u < 4; ++u) { da = mfma(A2[u], B2[u], da); db = mfma(A2[u], B2[u + 4], db); }
        st16(rws, u32x4{asu(da[0]), tag, asu(db[0]), tag}, spo, odd ? WS_FP : WS_SP);
        SB;
    }
    if (odd && ci < 56) {
        const u32 o0 = hblk + (j >> 1) * SP_STRIDE + c * 2048 + l * 16;
        const u32 o1 = c == 3 ? OOR : o0 + 1024, tg1 = c == 3 ? 0u : tag;
        u32 lf = L_FIN + ((l & 3) * 2 + (l >> 5)) * 16 + c * 4;
        PIN(lf);
        u32x4 g0, g1, g2, g3;
        float gate;
#define FLOAD g2 = ld16<AUX_SC1>(rws, o0, WS_FP); g3 = ld16<AUX_SC1>(rws, o1, WS_FP)
#define PLOAD g0 = ld16<AUX_SC1>(rws, o0, WS_SP); g1 = ld16<AUX_SC1>(rws, o0, WS_SP + 1024)
        UNTIL(GX);
        FLOAD; SB;
        {
            POLL(none(bad(g2, tag) | bad(g3, tg1)), FLOAD)
            float f = tr32(asf(g2[0]) + asf(g3[0]), asf(g2[2]) + asf(g3[2]));
            f = tr16(f, f); f += dpp<ROR8>(f); f += dpp<ROR4>(f);
            LD(float, lf + 128) = f;
            bar();
            const f32x4 r = LD(f32x4, L_FIN + 128 + (l & 7) * 16);
            gate = rbf(silu(rbf(r[0] + r[1] + r[2] + r[3])));
            PIN(gate);
        }
        __builtin_amdgcn_s_sleep(FD);
        PLOAD;
        POLL(none(bad(g0, tag) | bad(g1, tag)), PLOAD)
        float s = tr32(asf(g0[0]) + asf(g1[0]), asf(g0[2]) + asf(g1[2]));
        s = tr16(s, s); s += dpp<ROR8>(s); s += dpp<ROR4>(s);
        LD(float, lf) = s;
        bar();
        if (c == 0) {
            const f32x4 r = LD(f32x4, L_FIN + (l & 7) * 16);
            const float y = blo(xo) + rbf(rbf(r[0] + r[1] + r[2] + r[3]) * gate);
            __builtin_amdgcn_raw_buffer_store_b16(__builtin_bit_cast(u16, (__bf16)y), mk(a_out, D * 2), l < 8 ? ocol * 2 : OOR, 0, 0);
            if (ci == 0) __builtin_amdgcn_raw_buffer_store_b32(tag, rws, l == 0 ? 0u : OOR, 0, AUX_SC1);
        }
    }
}

extern "C" void kernel_launch(void* const* d_in, const int* in_sizes, int n_in, void* d_out, int out_size, void* d_ws, size_t ws_size, hipStream_t stream) {
    fused_kernel<<<256, 320, 0, stream>>>(d_in[0], d_ws, d_in[1], d_in[2], d_out, d_in[4], d_in[8], d_in[3], d_in[6], d_in[7]);
}
```

```cpp
#include <hip/hip_runtime.h>
#include <hip/hip_bf16.h>
#include <stdint.h>

typedef unsigned short u16;
typedef unsigned int u32;
typedef unsigned long long u64;
typedef u32 u32x4 __attribute__((ext_vector_type(4)));
typedef u32 u32x2 __attribute__((ext_vector_type(2)));
typedef u64 u64x4 __attribute__((ext_vector_type(4)));
typedef float f32x2 __attribute__((ext_vector_type(2)));
typedef float f32x4 __attribute__((ext_vector_type(4)));
typedef __bf16 bf16x2 __attribute__((ext_vector_type(2)));
typedef __bf16 bf16x8 __attribute__((ext_vector_type(8)));
typedef short s16x4 __attribute__((ext_vector_type(4)));
typedef __amdgpu_buffer_rsrc_t rsrc_t;
#define LAS __attribute__((address_space(3)))

constexpr int D = 448, SK = 65536, DH = 256, WIN_KEYS = 4096, F = 1792;
constexpr u32 WSBLK = 262144, SP_STRIDE = 8192;
constexpr u32 WS_QP = 256;
constexpr u32 WS_ST = WS_QP + 4096;
constexpr u32 WS_GU = WS_ST + 1024;
constexpr u32 WS_OP = WS_GU + 8192;
constexpr u32 WS_SP = WS_OP + 65536;
constexpr u32 WS_FP = WS_SP + 7 * SP_STRIDE;
constexpr u32 WS_END = 8 * WSBLK;
static_assert(WS_FP + 7 * SP_STRIDE <= WSBLK, "workspace block");
constexpr u32 L_K = 0, L_V = 65536, L_W2 = 131072, L_XN = L_W2 + 28672, L_Q = L_XN + 1024, L_P = L_Q + 1024, L_ML = L_P + 512, L_A = L_ML + 64,
              L_FIN = L_A + 64, L_FLAG = L_FIN + 256, L_DBG = L_FLAG + 64, L_END = L_DBG + 128;
constexpr u32 F_PROG = L_FLAG, F_GATE = L_FLAG + 4, F_CQ = L_FLAG + 8, F_CS = L_FLAG + 12, F_C1 = L_FLAG + 16, L_ZERO = L_FLAG + 32;
constexpr int AUX_NT = 2, AUX_SC1 = 16;
constexpr u32 SPIN_CAP = 1u << 18, OOR = 0x80000000u;
constexpr int PACE0 = 72;
constexpr int PACE_DT = 232;
constexpr int WIN = 36;
constexpr int QWAIT = 50;
constexpr int QX = 292;
constexpr int SX = 612;
constexpr int HWAIT = 20, HX = 672;
constexpr int GX = 732;
constexpr int FD = 8;
constexpr int CODE_KB = 11;

#define SB __builtin_amdgcn_sched_barrier(0)
#define PIN(x) asm volatile("" : "+v"(x))
__device__ __forceinline__ rsrc_t mk(u64 p, u32 bytes) { return __builtin_amdgcn_make_buffer_rsrc((void*)p, 0, bytes, 0x00020000); }
template <int AUX> __device__ __forceinline__ u32x4 ld16(rsrc_t r, u32 voff, u32 soff) { return __builtin_amdgcn_raw_buffer_load_b128(r, voff, soff, AUX); }
__device__ __forceinline__ void st16(rsrc_t r, u32x4 v, u32 voff, u32 soff) { __builtin_amdgcn_raw_buffer_store_b128(v, r, voff, soff, AUX_SC1); }
__device__ __forceinline__ void st8(rsrc_t r, u32x2 v, u32 voff, u32 soff) { __builtin_amdgcn_raw_buffer_store_b64(v, r, voff, soff, AUX_SC1); }
__device__ __forceinline__ u32 now() { return (u32)__builtin_amdgcn_s_memrealtime(); }
__device__ __forceinline__ float asf(u32 v) { return __builtin_bit_cast(float, v); }
__device__ __forceinline__ u32 asu(float v) { return __builtin_bit_cast(u32, v); }
__device__ __forceinline__ float blo(u32 v) { return asf(v << 16); }
__device__ __forceinline__ float bhi(u32 v) { return asf(v & 0xffff0000u); }
__device__ __forceinline__ float rbf(float f) { return (float)(__bf16)f; }
__device__ __forceinline__ u32 pk(float a, float b) { return __builtin_bit_cast(u32, bf16x2{(__bf16)a, (__bf16)b}); }
__device__ __forceinline__ float dot2(u32 a, u32 b, float c) { return __builtin_amdgcn_fdot2_f32_bf16(__builtin_bit_cast(bf16x2, a), __builtin_bit_cast(bf16x2, b), c, false); }
__device__ __forceinline__ f32x4 mfma(u32x4 a, u32x4 b, f32x4 c) { return __builtin_amdgcn_mfma_f32_16x16x32_bf16(__builtin_bit_cast(bf16x8, a), __builtin_bit_cast(bf16x8, b), c, 0, 0, 0); }
template <int CTRL> __device__ __forceinline__ float dpp(float x) { return asf(__builtin_amdgcn_mov_dpp((int)asu(x), CTRL, 0xf, 0xf, true)); }
constexpr int XOR1 = 0xB1, XOR2 = 0x4E, ROR8 = 0x128, ROR4 = 0x124;
__device__ __forceinline__ float tr32(float x, float y) { auto s = __builtin_amdgcn_permlane32_swap(asu(x), asu(y), false, false); return asf(s[0]) + asf(s[1]); }
__device__ __forceinline__ float tr16(float x, float y) { auto s = __builtin_amdgcn_permlane16_swap(asu(x), asu(y), false, false); return asf(s[0]) + asf(s[1]); }
__device__ __forceinline__ float fmx(float a, float b) { return __builtin_elementwise_maximum(a, b); }
__device__ __forceinline__ float max32(float x) { auto s = __builtin_amdgcn_permlane32_swap(asu(x), asu(x), false, false); return fmx(asf(s[0]), asf(s[1])); }
__device__ __forceinline__ float max16(float x) { auto s = __builtin_amdgcn_permlane16_swap(asu(x), asu(x), false, false); return fmx(asf(s[0]), asf(s[1])); }
__device__ __forceinline__ float sum_hi3(float x) { x += dpp<ROR8>(x); x = tr16(x, x); return tr32(x, x); }
__device__ __forceinline__ float sum_all(float x) { x += dpp<XOR1>(x); x += dpp<XOR2>(x); x += dpp<ROR4>(x); return sum_hi3(x); }
__device__ __forceinline__ float max_hi3(float x) { x = fmx(x, dpp<ROR8>(x)); return max32(max16(x)); }
__device__ __forceinline__ float sel4(u32 g, float a, float b, float c, float d) { return g & 2 ? (g & 1 ? d : c) : (g & 1 ? b : a); }
__device__ __forceinline__ float silu(float v) { return v * __builtin_amdgcn_rcpf(1.f + __expf(-v)); }
__device__ __forceinline__ void bar() { SB; asm volatile("s_waitcnt lgkmcnt(0)\n\ts_barrier" ::: "memory"); SB; }
__device__ __forceinline__ u32 bad(u32x4 g, u32 tag) { return (g[1] ^ tag) | (g[3] ^ tag); }
__device__ __forceinline__ u32 bad4(u32x4 g, u32 tag) {
    return __builtin_amdgcn_perm(__builtin_amdgcn_perm(g[3], g[2], 0x0c0c0400u), __builtin_amdgcn_perm(g[1], g[0], 0x0c0c0400u), 0x05040100u) ^ tag;
}
__device__ __forceinline__ u32x4 enc4(f32x4 v, u32 tag) {
    return u32x4{((asu(v[0]) + 0x80u) & ~0xffu) | (tag & 0xffu), ((asu(v[1]) + 0x80u) & ~0xffu) | ((tag >> 8) & 0xffu),
                 ((asu(v[2]) + 0x80u) & ~0xffu) | ((tag >> 16) & 0xffu), ((asu(v[3]) + 0x80u) & ~0xffu) | (tag >> 24)};
}
__device__ __forceinline__ float v24(u32 v) { return asf(v & ~0xffu); }
__device__ __forceinline__ bool none(u32 v) { PIN(v); return __builtin_amdgcn_ballot_w64(v != 0) == 0; }
template <int N> __device__ __forceinline__ void waitvm() { asm volatile("s_waitcnt vmcnt(%0)" :: "n"(N) : "memory"); }
__device__ __forceinline__ void dma4(rsrc_t r, u32 v0, u32 v1, u32 v2, u32 v3, u32 soff, u32 ldsb) {
    asm volatile("s_mov_b32 m0, %6\n\ts_nop 0\n\t"
                 "buffer_load_dwordx4 %0, %4, %5 offen nt lds\n\t"
                 "buffer_load_dwordx4 %1, %4, %5 offen offset:1024 nt lds\n\t"
                 "buffer_load_dwordx4 %2, %4, %5 offen offset:2048 nt lds\n\t"
                 "buffer_load_dwordx4 %3, %4, %5 offen offset:3072 nt lds"
                 :: "v"(v0), "v"(v1), "v"(v2), "v"(v3), "s"(r), "s"(soff), "s"(ldsb) : "memory");
}
__device__ __forceinline__ u32x4 trfrag(LAS char* p, u32 rstep) {
    const u32x2 lo = __builtin_bit_cast(u32x2, __builtin_amdgcn_ds_read_tr16_b64_v4i16((LAS s16x4*)p));
    const u32x2 hi = __builtin_bit_cast(u32x2, __builtin_amdgcn_ds_read_tr16_b64_v4i16((LAS s16x4*)(p + rstep)));
    return u32x4{lo[0], lo[1], hi[0], hi[1]};
}
#define POLL(COND, RELOAD) u32 spins = 0; if (__builtin_expect(!(COND), 0)) { do { asm volatile("" ::: "memory"); RELOAD; ++spins; } while (!(COND) && spins < SPIN_CAP); }

#define STAMP(i)

__global__ void __launch_bounds__(320, 2) fused_kernel(const void*, void*, const void*, const void*, void*, const void*, const void*, const void*, const void*, const void*) {
    __shared__ __attribute__((aligned(1024))) char lds_[L_END];
    LAS char* lds = (LAS char*)lds_;
#define LD(T, off) (*(LAS T*)(lds + (off)))
#define FLAG(off) __builtin_amdgcn_readfirstlane(*(volatile LAS u32*)(lds + (off)))
#define SETFLAG(off, v) (*(volatile LAS u32*)(lds + (off)) = (v))
#define LWAIT(COND) do { u32 sp_ = 0; while (!(COND) && ++sp_ < (SPIN_CAP << 3)) __builtin_amdgcn_s_sleep(1); asm volatile("" ::: "memory"); } while (0)
#define LSIGNAL(off) do { asm volatile("s_waitcnt lgkmcnt(0)" ::: "memory"); if (l == 0) __hip_atomic_fetch_add((LAS u32*)(lds + (off)), 1u, __ATOMIC_RELAXED, __HIP_MEMORY_SCOPE_WORKGROUP); } while (0)
    u32 k0, k1, k2;
    asm volatile("s_load_dword %0, %3, 0x0\n\ts_load_dword %1, %3, 0x40\n\ts_load_dword %2, %3, 0x4c" : "=&s"(k0), "=&s"(k1), "=&s"(k2) : "s"(__builtin_amdgcn_kernarg_segment_ptr()));
    SB;
    const u32 b = blockIdx.x, h = b & 7, j = b >> 3;
    const u32 l = threadIdx.x & 63, w = __builtin_amdgcn_readfirstlane(threadIdx.x >> 6);
    const bool odd = j & 1;
    const u32 cgf = h + 8 * (j >> 3), rgf = (j >> 1) & 3;
    const bool ffn = cgf < 28;
    u64x4 A;
    u64 a_out, a_w;
#define LOAD_ARGS(WOFF, ...) asm volatile("s_load_dwordx8 %0, %3, 0x0\n\ts_load_dwordx2 %2, %3, %4\n\ts_load_dwordx2 %1, %3, 0x20\n\ts_waitcnt lgkmcnt(0)" \
                 : "=&s"(A), "=&s"(a_out), "=&s"(a_w) : "s"(__builtin_amdgcn_kernarg_segment_ptr()), "s"(WOFF), "s"(k0), "s"(k1), "s"(k2), __VA_ARGS__)

    if (w == 0) {
        const u32 kvoff = (h * SK + (SK - WIN_KEYS) + j * 16) * (DH * 2);
        const u32 w2off = (odd ? cgf * 64 + rgf * 16 : h * 512 + j * 8) * (D * 2);
        const u32 w2bytes = odd ? (ffn ? 16 * D * 2 : 0) : 272 * D * 2;
        const u32 hb = l >> 5;
        const u32 vk = hb * 512 + (((l & 31) ^ hb) << 4), vv = hb * 512 + (((l & 31) ^ (hb << 1)) << 4);
        const u32 lb = (u32)(uintptr_t)lds;
        LD(u32, L_FLAG + (l & 15) * 4) = 0u;
        LOAD_ARGS(0x28 + (j & 1) * 8, "v"(vk), "v"(vv), "s"(kvoff), "s"(w2off), "s"(w2bytes));
        u32 due = (now() + PACE0) << 4;
        const rsrc_t rk = mk(A[2] + kvoff, 0x1c2000), rv = mk(A[3] + kvoff, 0x1c2000), r2 = mk(a_w + w2off, w2bytes);
        bar();
        STAMP(1);
#define PACE do { while ((int)((now() << 4) - due) < 0) ; due += PACE_DT; } while (0)
#pragma unroll 1
        for (u32 g = 0; g < 8; ++g) {
            const u32 so = ((g + j) & 7) << 18;
            PACE;
            dma4(rk, vk, vk ^ 32, vk ^ 64, vk ^ 96, so, lb + L_K + g * 8192);
            waitvm<WIN>();
            PACE;
            dma4(rk, vk ^ 128, vk ^ 160, vk ^ 192, vk ^ 224, so + 4096, lb + L_K + g * 8192 + 4096);
            waitvm<WIN>();
        }
        STAMP(2);
#pragma unroll 1
        for (u32 g = 0; g < 8; ++g) {
            const u32 so = ((g + j) & 7) << 18;
            PACE;
            dma4(rv, vv, vv ^ 64, vv, vv ^ 64, so, lb + L_V + g * 8192);
            waitvm<WIN>();
            PACE;
            dma4(rv, vv ^ 128, vv ^ 192, vv ^ 128, vv ^ 192, so + 4096, lb + L_V + g * 8192 + 4096);
            if (g & 1) waitvm<WIN>();
            else { waitvm<24>(); SETFLAG(F_PROG, g * 8 + 48); }
        }
        STAMP(3);
#define W2G(g) do { const u32 vo = l * 16 + (g) * 4096 + ((g) > 3 ? 240 * D * 2 : 0), vp = l * 16 + (g) * 4096 + ((g) > 2 ? 240 * D * 2 : 0); \
                    dma4(r2, vo, vo, vp, vp, 0, lb + L_W2 + (g) * 4096); } while (0)
        PACE; W2G(0);
        PACE; W2G(1);
        waitvm<24>(); SETFLAG(F_PROG, 112);
        PACE; W2G(2);
        PACE; W2G(3);
        waitvm<16>();
        STAMP(4);
        bar();
        W2G(4); W2G(5); W2G(6);
        waitvm<0>();
        SETFLAG(F_PROG, 156);
        STAMP(5);
        return;
    }

    const u32 c = w - 1, g = l >> 4;
    const u32 row0 = (odd ? rgf * 2 + (c & 1) : (j >> 4) * 4 + c) * 56;
    const u32 wstride = odd ? F * 2 : 8192;
    const u32 voff = (row0 + 2 * (l >> 3)) * wstride + (odd ? cgf : h * 8 + ((j >> 1) & 7)) * 128 + (l & 7) * 16;
    const u32 voff3 = l < 32 ? voff : OOR;
    const u32 w1bytes = odd ? (ffn ? D * F * 2 : 0) : D * 4096 * 2;
    const u32 ci = (j >> 1) * 8 + h;
    const u32 ocol = (ci >> 4) * 128 + (ci & 15) * 4 + ((l >> 1) & 3) + (l & 1) * (ci >= 48 ? 32 : 64);
    const u32x4 cw = ld16<0>(mk(__builtin_amdgcn_s_getpc() & ~(u64)255, c == 3 ? CODE_KB * 1024 : 0), j * 1024 + l * 16, 0);
    LOAD_ARGS(odd ? 0x40 + (c >> 1) * 8 : 0x38, "v"(voff), "v"(voff3), "v"(ocol), "s"(w1bytes), "s"(wstride));
    const rsrc_t rws = mk(A[1], WS_END);
    const u32 hblk = h * WSBLK;
    const u32 ep = *(const __attribute__((address_space(4))) u32*)A[1];
    SB;
    const u32 tb = now();
    u32 tag, xo;
    SB;
#define UNTIL(T) while ((int)(now() - tb) < (T)) __builtin_amdgcn_s_sleep(1)
    {
        u32x4 W[8];
        const u32x4 xr = ld16<0>(mk(A[0], D * 2), l * 16, 0);
        SB;
        const rsrc_t rw = mk(a_w, w1bytes);
#pragma unroll
        for (int i = 0; i < 8; ++i) { W[i] = ld16<AUX_NT>(rw, i < 6 ? voff : voff3, ((i >> 1) * 16 + (i & 1)) * wstride); SB; }
        asm volatile("s_barrier" ::: "memory"); SB;
        xo = __builtin_amdgcn_raw_buffer_load_b16(mk(A[0], D * 2), ocol * 2, 0, 0);
        SB;
        STAMP(1);
        asm volatile("" :: "v"(cw));
        float xv[8];
#pragma unroll
        for (int e = 0; e < 4; ++e) { xv[2 * e] = blo(xr[e]); xv[2 * e + 1] = bhi(xr[e]); }
        float s = 0.f;
#pragma unroll
        for (int e = 0; e < 8; ++e) s += xv[e];
        const float mean = sum_all(s) * (1.f / D);
        float q = 0.f;
#pragma unroll
        for (int e = 0; e < 8; ++e) { const float d = xv[e] - mean; q += d * d; }
        q = l < 56 ? q : 0.f;
        const float var = rbf(sum_all(q) * (1.f / D));
        const float mb = rbf(mean);
        const float rd = 1.f / rbf(__builtin_amdgcn_sqrtf(rbf(var + 1e-5f)));
        u32x4 xn;
#pragma unroll
        for (int e = 0; e < 4; ++e) xn[e] = pk(rbf(xv[2 * e] - mb) * rd, rbf(xv[2 * e + 1] - mb) * rd);
        if (l >= 56) xn = u32x4{0u, 0u, 0u, 0u};
        LD(u32x4, L_XN + l * 16) = xn;
        float a[8];
#pragma unroll
        for (int e = 0; e < 8; ++e) a[e] = 0.f;
#pragma unroll
        for (int i = 0; i < 4; ++i) {
            const u32 xp = LD(u32, L_XN + (row0 + 16 * i + 2 * (l >> 3)) * 2);
#pragma unroll
            for (int e = 0; e < 4; ++e) {
                a[2 * e] = dot2(__builtin_amdgcn_perm(W[2 * i + 1][e], W[2 * i][e], 0x05040100u), xp, a[2 * e]);
                a[2 * e + 1] = dot2(__builtin_amdgcn_perm(W[2 * i + 1][e], W[2 * i][e], 0x07060302u), xp, a[2 * e + 1]);
            }
        }
#pragma unroll
        for (int e = 0; e < 4; ++e) a[e] = tr32(a[e], a[e + 4]);
        a[0] = tr16(a[0], a[2]); a[1] = tr16(a[1], a[3]);
        a[0] += dpp<ROR8>(a[0]); a[1] += dpp<ROR8>(a[1]);
        LD(f32x2, L_Q + c * 256 + (l & 7) * 32 + g * 8) = f32x2{a[0], a[1]};
        LSIGNAL(F_C1);
        tag = ((ep & 0x7f7f7f7fu) + 0x01010101u) ^ (ep & 0x80808080u);
        if (c == 0) {
            u32 so = odd ? (((j >> 3) * 4 + ((l & 15) >> 2)) * 4 + rgf) * 128 + (l & 16) * 4 + (l & 3) * 16 + WS_GU : (j >> 4) * 2048 + ((j >> 1) & 7) * 256 + l * 16 + WS_QP;
            so = l >= (odd ? (ffn ? 32u : 0u) : 16u) ? OOR : so;
            PIN(so);
            LWAIT(FLAG(F_C1) == 4);
            f32x4 v = LD(f32x4, L_Q + (l & 16) * 32 + (l & 15) * 16) + LD(f32x4, L_Q + (l & 16) * 32 + 256 + (l & 15) * 16);
            if (!odd) {
#pragma unroll
                for (int e = 0; e < 4; ++e) v[e] = tr16(v[e], v[e]);
            }
            st16(rws, enc4(v, tag), so, hblk);
            SETFLAG(F_GATE, (now() + (odd ? 0 : QWAIT)) | 1u);
        }
    }
    STAMP(3);
    { u32 gate; LWAIT((gate = FLAG(F_GATE)) != 0); LWAIT((int)(now() - gate) >= 0); UNTIL(QX); }
    STAMP(4);
    u32x4 gu;
    const bool hw = odd && ffn && c == 1;
    {
        u32x4 qg;
#define QLOAD qg = ld16<AUX_SC1>(rws, (l >> 5) * 2048 + c * 512 + (l & 31) * 16, hblk + WS_QP)
#define GLOAD gu = ld16<AUX_SC1>(rws, (l & 31) * 16, hblk + WS_GU + ((j >> 3) * 4 + rgf) * 512)
        QLOAD;
        {
            POLL(none(bad4(qg, tag)), QLOAD)
        }
        LD(u32, L_Q + c * 256 + (l & 31) * 8 + (l >> 5) * 4) = pk(tr32(v24(qg[0]), v24(qg[2])), tr32(v24(qg[1]), v24(qg[3])));
        LSIGNAL(F_CQ);
        if (hw) { GLOAD; }
        STAMP(5);
        const u32 r = l & 15;
        u32x4 kf[16], qa[8];
        LWAIT(FLAG(F_PROG) >= 64);
#pragma unroll
        for (int s = 0; s < 8; ++s) {
            const u32 ko = L_K + (c * 32 + r) * 512 + (((4 * (s & 3) + g) ^ r) << 4) + (s >> 2) * 256;
            kf[2 * s] = LD(u32x4, ko); kf[2 * s + 1] = LD(u32x4, ko + 8192);
        }
        SB;
        LWAIT(FLAG(F_CQ) == 4);
#pragma unroll
        for (int s = 0; s < 8; ++s) qa[s] = LD(u32x4, L_Q + (l & 1) * 512 + s * 64 + g * 16);
        SB;
        f32x4 d0 = {0.f, 0.f, 0.f, 0.f}, d1 = d0;
#pragma unroll
        for (int s = 0; s < 8; ++s) { d0 = mfma(qa[s], kf[2 * s], d0); d1 = mfma(qa[s], kf[2 * s + 1], d1); }
        const float sv = rbf(sel4(g, d0[0], d1[0], d0[1], d1[1])) * (0.0625f * 1.4426950408889634f);
        float m = fmx(sv, dpp<XOR1>(sv));
        m = fmx(m, dpp<XOR2>(m)); m = fmx(m, dpp<ROR4>(m)); m = fmx(m, dpp<ROR8>(m)); m = max16(m);
        const __bf16 p = (__bf16)__builtin_amdgcn_exp2f(sv - m);
        LD(__bf16, L_P + (l >> 5) * 256 + c * 64 + (l & 31) * 2) = p;
        float ls = (float)p;
        ls += dpp<XOR1>(ls); ls += dpp<XOR2>(ls); ls += dpp<ROR4>(ls); ls += dpp<ROR8>(ls); ls = tr16(ls, ls);
        if ((l & 31) == 0) { LD(float, L_ML + (l >> 5) * 16 + c * 4) = m; LD(float, L_ML + 32 + (l >> 5) * 16 + c * 4) = ls; }
        LSIGNAL(F_CS);
    }
    LWAIT(FLAG(F_CS) == 4);
    const bool cw0 = !odd && c == 0;
    u32x4 sg[4];
#define SLOAD _Pragma("unroll") for (int i = 0; i < 4; ++i) sg[i] = ld16<AUX_SC1>(rws, (l >> 3) * 16, hblk + WS_ST + i * 128)
    {
        u32x4 PA[4];
        f32x4 f0, f1;
#pragma unroll
        for (int s = 0; s < 4; ++s) PA[s] = LD(u32x4, L_P + (l & 1) * 256 + s * 64 + g * 16);
        const f32x4 m0 = LD(f32x4, L_ML), m1 = LD(f32x4, L_ML + 16), l0 = LD(f32x4, L_ML + 32), l1 = LD(f32x4, L_ML + 48);
        const float M0 = fmx(fmx(m0[0], m0[1]), fmx(m0[2], m0[3])), M1 = fmx(fmx(m1[0], m1[1]), fmx(m1[2], m1[3]));
#pragma unroll
        for (int s = 0; s < 4; ++s) { f0[s] = __builtin_amdgcn_exp2f(m0[s] - M0); f1[s] = __builtin_amdgcn_exp2f(m1[s] - M1); }
        if (c == 3)
            st16(rws, enc4(f32x4{M0, f0[0] * l0[0] + f0[1] * l0[1] + f0[2] * l0[2] + f0[3] * l0[3], M1, f1[0] * l1[0] + f1[1] * l1[1] + f1[2] * l1[2] + f1[3] * l1[3]}, tag),
                 l == 0 ? j * 16 : OOR, hblk + WS_ST);
        STAMP(7);
        const u32 q = (l >> 2) & 3, f = q | ((g & 1) << 2);
        u32 vo[4], pg = 0, opo = (c * 64 + l) * 8;
        PIN(opo);
        float v0 = 0.f, v1 = 0.f;
#pragma unroll
        for (int u = 0; u < 4; ++u) vo[u] = L_V + (8 * g + q) * 512 + (((4 * c + u) ^ f) << 5) + (l & 3) * 8;
#pragma unroll
        for (int s = 0; s < 4; ++s) {
            if (s < 3) { if (pg < 80 + 16 * s) LWAIT((pg = FLAG(F_PROG)) >= 80 + 16 * s); }
            else {
                if (hw) {
                    POLL(none(bad4(gu, tag)), GLOAD)
                    float hv[4];
#pragma unroll
                    for (int e = 0; e < 4; ++e) {
                        float x = v24(gu[e]);
                        x += dpp<ROR8>(x); x = rbf(tr16(x, x));
                        hv[e] = rbf(silu(x)) * dpp<ROR4>(x);
                    }
                    if (l < 8) LD(u32x2, L_A + l * 8) = l < 4 ? u32x2{pk(hv[0], hv[1]), pk(hv[2], hv[3])} : u32x2{0u, 0u};
                }
                bar();
            }
            f32x4 d[4];
#pragma unroll
            for (int u = 0; u < 4; ++u) d[u] = mfma(PA[s], trfrag(lds + vo[u] + s * 16384, 2048), f32x4{0.f, 0.f, 0.f, 0.f});
            v0 = fmaf(f0[s], sel4(g, d[0][0], d[1][0], d[2][0], d[3][0]), v0);
            v1 = fmaf(f1[s], sel4(g, d[0][1], d[1][1], d[2][1], d[3][1]), v1);
        }
        st8(rws, u32x2{pk(v0, v1), tag}, opo, hblk + WS_OP + j * 2048);
        SB;
    }
    STAMP(8);
    u32x4 og[4];
    f32x2 wp[4];
#define OLOAD _Pragma("unroll") for (int i = 0; i < 4; ++i) og[i] = ld16<AUX_SC1>(rws, (l >> 3) * 2048 + (l & 7) * 16, hblk + WS_OP + i * 16384 + (j >> 1) * 128)
    u32 spo, bo, ao[4], la = L_A + ((l >> 5) * 8 + (l & 7)) * 4;
    {
        const u32 cc = c * 16 + (l >> 2);
        spo = cc < 56 ? (cc & 7) * WSBLK + (cc >> 3) * SP_STRIDE + (odd ? cgf * 4 + rgf : ci) * 64 + (l & 3) * 16 : OOR;
        bo = L_W2 + (((8 * g) & (odd ? 15 : 31)) + ((l >> 2) & 3)) * 896 + c * 256 + (l & 3) * 8;
#pragma unroll
        for (int u = 0; u < 4; ++u) { ao[u] = ((l >> 2) & 3) == u ? L_A + g * 16 : L_ZERO; PIN(ao[u]); }
        PIN(spo); PIN(bo); PIN(la);
    }
    if (cw0) {
        UNTIL(SX);
        SLOAD; SB;
        { const u32 tw = now() + HWAIT; while ((int)(now() - tw) < 0) ; }
        UNTIL(HX);
        OLOAD; SB;
        POLL(none(bad4(sg[0], tag) | bad4(sg[1], tag) | bad4(sg[2], tag) | bad4(sg[3], tag)), SLOAD)
#pragma unroll
        for (int hd = 0; hd < 2; ++hd) {
            const float M = max_hi3(fmx(fmx(v24(sg[0][2 * hd]), v24(sg[1][2 * hd])), fmx(v24(sg[2][2 * hd]), v24(sg[3][2 * hd]))));
            float L = 0.f;
#pragma unroll
            for (int i = 0; i < 4; ++i) { wp[i][hd] = __builtin_amdgcn_exp2f(v24(sg[i][2 * hd]) - M); L = fmaf(wp[i][hd], v24(sg[i][2 * hd + 1]), L); }
            const float rl = __builtin_amdgcn_rcpf(sum_hi3(L));
#pragma unroll
            for (int i = 0; i < 4; ++i) wp[i][hd] *= rl;
        }
        PIN(wp[0]); PIN(wp[1]); PIN(wp[2]); PIN(wp[3]); SB;
    }
    LWAIT(FLAG(F_PROG) == 156);
    STAMP(9);
    u32x4 B2[8];
#pragma unroll
    for (int u = 0; u < 8; ++u) B2[u] = trfrag(lds + bo - (c == 3 ? ((u + 2) >> 2) * 64 : 0) + u * 32, 4 * 896);
    if (!odd) {
        if (c == 0) {
            POLL(none(bad(og[0], tag) | bad(og[1], tag) | bad(og[2], tag) | bad(og[3], tag)), OLOAD)
            STAMP(10);
            f32x2 a0 = {0.f, 0.f}, a1 = a0;
#pragma unroll
            for (int i = 0; i < 4; ++i) {
                a0 = __builtin_elementwise_fma(wp[i], f32x2{blo(og[i][0]), bhi(og[i][0])}, a0);
                a1 = __builtin_elementwise_fma(wp[i], f32x2{blo(og[i][2]), bhi(og[i][2])}, a1);
            }
            float o0 = tr32(a0[0], a0[1]), o1 = tr32(a1[0], a1[1]);
            o0 = tr16(o0, o0); o1 = tr16(o1, o1);
            o0 += dpp<ROR8>(o0); o1 += dpp<ROR8>(o1);
            LD(u32, la) = pk(o0, o1);
        }
        bar();
    }
    if (!odd || ffn) {
        f32x4 da = {0.f, 0.f, 0.f, 0.f}, db = da;
        u32x4 A2[4];
#pragma unroll
        for (int u = 0; u < 4; ++u) A2[u] = LD(u32x4, ao[u]);
#pragma unroll
        for (int u = 0; u < 4; ++u) { da = mfma(A2[u], B2[u], da); db = mfma(A2[u], B2[u + 4], db); }
        st16(rws, u32x4{asu(da[0]), tag, asu(db[0]), tag}, spo, odd ? WS_FP : WS_SP);
        SB;
    }
    STAMP(11);
    if (odd && ci < 56) {
        const u32 o0 = hblk + (j >> 1) * SP_STRIDE + c * 2048 + l * 16;
        const u32 o1 = c == 3 ? OOR : o0 + 1024, tg1 = c == 3 ? 0u : tag;
        u32 lf = L_FIN + ((l & 3) * 2 + (l >> 5)) * 16 + c * 4;
        PIN(lf);
        u32x4 g0, g1, g2, g3;
        float gate;
#define FLOAD g2 = ld16<AUX_SC1>(rws, o0, WS_FP); g3 = ld16<AUX_SC1>(rws, o1, WS_FP)
#define PLOAD g0 = ld16<AUX_SC1>(rws, o0, WS_SP); g1 = ld16<AUX_SC1>(rws, o0, WS_SP + 1024)
        UNTIL(GX);
        FLOAD; SB;
        {
            POLL(none(bad(g2, tag) | bad(g3, tg1)), FLOAD)
            float f = tr32(asf(g2[0]) + asf(g3[0]), asf(g2[2]) + asf(g3[2]));
            f = tr16(f, f); f += dpp<ROR8>(f); f += dpp<ROR4>(f);
            LD(float, lf + 128) = f;
            bar();
            const f32x4 r = LD(f32x4, L_FIN + 128 + (l & 7) * 16);
            gate = rbf(silu(rbf(r[0] + r[1] + r[2] + r[3])));
            PIN(gate);
        }
        STAMP(10);
        __builtin_amdgcn_s_sleep(FD);
        PLOAD;
        POLL(none(bad(g0, tag) | bad(g1, tag)), PLOAD)
        STAMP(12);
        float s = tr32(asf(g0[0]) + asf(g1[0]), asf(g0[2]) + asf(g1[2]));
        s = tr16(s, s); s += dpp<ROR8>(s); s += dpp<ROR4>(s);
        LD(float, lf) = s;
        bar();
        if (c == 0) {
            const f32x4 r = LD(f32x4, L_FIN + (l & 7) * 16);
            const float y = blo(xo) + rbf(rbf(r[0] + r[1] + r[2] + r[3]) * gate);
            __builtin_amdgcn_raw_buffer_store_b16(__builtin_bit_cast(u16, (__bf16)y), mk(a_out, D * 2), l < 8 ? ocol * 2 : OOR, 0, 0);
            if (ci == 0) __builtin_amdgcn_raw_buffer_store_b32(tag, rws, l == 0 ? 0u : OOR, 0, AUX_SC1);
        }
    }
}

extern "C" void kernel_launch(void* const* d_in, const int* in_sizes, int n_in, void* d_out, int out_size, void* d_ws, size_t ws_size, hipStream_t stream) {
    fused_kernel<<<256, 320, 0, stream>>>(d_in[0], d_ws, d_in[1], d_in[2], d_out, d_in[4], d_in[8], d_in[3], d_in[6], d_in[7]);
}
```

```cpp
#include <hip/hip_runtime.h>
#include <hip/hip_bf16.h>
#include <stdint.h>

typedef unsigned short u16;
typedef unsigned int u32;
typedef unsigned long long u64;
typedef u32 u32x4 __attribute__((ext_vector_type(4)));
typedef u32 u32x2 __attribute__((ext_vector_type(2)));
typedef u64 u64x4 __attribute__((ext_vector_type(4)));
typedef float f32x2 __attribute__((ext_vector_type(2)));
typedef float f32x4 __attribute__((ext_vector_type(4)));
typedef __bf16 bf16x2 __attribute__((ext_vector_type(2)));
typedef __bf16 bf16x8 __attribute__((ext_vector_type(8)));
typedef short s16x4 __attribute__((ext_vector_type(4)));
typedef __amdgpu_buffer_rsrc_t rsrc_t;
#define LAS __attribute__((address_space(3)))

constexpr int D = 448, SK = 65536, DH = 256, WIN_KEYS = 4096, F = 1792;
constexpr u32 WSBLK = 262144, SP_STRIDE = 8192;
constexpr u32 WS_QP = 256;
constexpr u32 WS_ST = WS_QP + 4096;
constexpr u32 WS_GU = WS_ST + 1024;
constexpr u32 WS_OP = WS_GU + 8192;
constexpr u32 WS_SP = WS_OP + 65536;
constexpr u32 WS_FP = WS_SP + 7 * SP_STRIDE;
constexpr u32 WS_END = 8 * WSBLK;
static_assert(WS_FP + 7 * SP_STRIDE <= WSBLK, "workspace block");
constexpr u32 L_K = 0, L_V = 65536, L_W2 = 131072, L_XN = L_W2 + 28672, L_Q = L_XN + 1024, L_P = L_Q + 1024, L_ML = L_P + 512, L_A = L_ML + 64,
              L_FIN = L_A + 64, L_FLAG = L_FIN + 256, L_DBG = L_FLAG + 64, L_END = L_DBG + 128;
constexpr u32 F_PROG = L_FLAG, F_GATE = L_FLAG + 4, F_CQ = L_FLAG + 8, F_CS = L_FLAG + 12, F_C1 = L_FLAG + 16, F_TE = L_FLAG + 20, F_TB = L_FLAG + 24, L_ZERO = L_FLAG + 32;
constexpr int AUX_NT = 2, AUX_SC1 = 16;
constexpr float SCL = 0.0625f * 1.4426950408889634f;
constexpr u32 SPIN_CAP = 1u << 18, OOR = 0x80000000u;
constexpr int PACE0 = 100;
constexpr int DT = 232;
constexpr int DT_WD = 64;
constexpr int WIN = 36;
constexpr int QWAIT = 50;
constexpr int QX = 292;
constexpr int SX = 90;
constexpr int HWAIT = 20, HX = 150;
constexpr int GX = 210;
constexpr int FD = 8;
constexpr int CODE_KB = 12;

#define SB __builtin_amdgcn_sched_barrier(0)
#define PIN(x) asm volatile("" : "+v"(x))
__device__ __forceinline__ rsrc_t mk(u64 p, u32 bytes) { return __builtin_amdgcn_make_buffer_rsrc((void*)p, 0, bytes, 0x00020000); }
template <int AUX> __device__ __forceinline__ u32x4 ld16(rsrc_t r, u32 voff, u32 soff) { return __builtin_amdgcn_raw_buffer_load_b128(r, voff, soff, AUX); }
__device__ __forceinline__ void st16(rsrc_t r, u32x4 v, u32 voff, u32 soff) { __builtin_amdgcn_raw_buffer_store_b128(v, r, voff, soff, AUX_SC1); }
__device__ __forceinline__ void st8(rsrc_t r, u32x2 v, u32 voff, u32 soff) { __builtin_amdgcn_raw_buffer_store_b64(v, r, voff, soff, AUX_SC1); }
__device__ __forceinline__ u32 now() { return (u32)__builtin_amdgcn_s_memrealtime(); }
__device__ __forceinline__ float asf(u32 v) { return __builtin_bit_cast(float, v); }
__device__ __forceinline__ u32 asu(float v) { return __builtin_bit_cast(u32, v); }
__device__ __forceinline__ float blo(u32 v) { return asf(v << 16); }
__device__ __forceinline__ float bhi(u32 v) { return asf(v & 0xffff0000u); }
__device__ __forceinline__ float rbf(float f) { return (float)(__bf16)f; }
__device__ __forceinline__ u32 pk(float a, float b) { return __builtin_bit_cast(u32, bf16x2{(__bf16)a, (__bf16)b}); }
__device__ __forceinline__ float dot2(u32 a, u32 b, float c) { return __builtin_amdgcn_fdot2_f32_bf16(__builtin_bit_cast(bf16x2, a), __builtin_bit_cast(bf16x2, b), c, false); }
__device__ __forceinline__ f32x4 mfma(u32x4 a, u32x4 b, f32x4 c) { return __builtin_amdgcn_mfma_f32_16x16x32_bf16(__builtin_bit_cast(bf16x8, a), __builtin_bit_cast(bf16x8, b), c, 0, 0, 0); }
template <int CTRL> __device__ __forceinline__ float dpp(float x) { return asf(__builtin_amdgcn_mov_dpp((int)asu(x), CTRL, 0xf, 0xf, true)); }
constexpr int XOR1 = 0xB1, XOR2 = 0x4E, ROR8 = 0x128, ROR4 = 0x124;
__device__ __forceinline__ float tr32(float x, float y) { auto s = __builtin_amdgcn_permlane32_swap(asu(x), asu(y), false, false); return asf(s[0]) + asf(s[1]); }
__device__ __forceinline__ float tr16(float x, float y) { auto s = __builtin_amdgcn_permlane16_swap(asu(x), asu(y), false, false); return asf(s[0]) + asf(s[1]); }
__device__ __forceinline__ float fmx(float a, float b) { return __builtin_elementwise_maximum(a, b); }
__device__ __forceinline__ float max32(float x) { auto s = __builtin_amdgcn_permlane32_swap(asu(x), asu(x), false, false); return fmx(asf(s[0]), asf(s[1])); }
__device__ __forceinline__ float max16(float x) { auto s = __builtin_amdgcn_permlane16_swap(asu(x), asu(x), false, false); return fmx(asf(s[0]), asf(s[1])); }
__device__ __forceinline__ float sum_hi3(float x) { x += dpp<ROR8>(x); x = tr16(x, x); return tr32(x, x); }
__device__ __forceinline__ float sum_all(float x) { x += dpp<XOR1>(x); x += dpp<XOR2>(x); x += dpp<ROR4>(x); return sum_hi3(x); }
__device__ __forceinline__ float max_hi3(float x) { x = fmx(x, dpp<ROR8>(x)); return max32(max16(x)); }
__device__ __forceinline__ float sel4(u32 g, float a, float b, float c, float d) { return g & 2 ? (g & 1 ? d : c) : (g & 1 ? b : a); }
__device__ __forceinline__ float silu(float v) { return v * __builtin_amdgcn_rcpf(1.f + __expf(-v)); }
__device__ __forceinline__ void bar() { SB; asm volatile("s_waitcnt lgkmcnt(0)\n\ts_barrier" ::: "memory"); SB; }
__device__ __forceinline__ u32 bad(u32x4 g, u32 tag) { return (g[1] ^ tag) | (g[3] ^ tag); }
__device__ __forceinline__ u32 bad4(u32x4 g, u32 tag) {
    return __builtin_amdgcn_perm(__builtin_amdgcn_perm(g[3], g[2], 0x0c0c0400u), __builtin_amdgcn_perm(g[1], g[0], 0x0c0c0400u), 0x05040100u) ^ tag;
}
__device__ __forceinline__ u32x4 enc4(f32x4 v, u32 tag) {
    return u32x4{((asu(v[0]) + 0x80u) & ~0xffu) | (tag & 0xffu), ((asu(v[1]) + 0x80u) & ~0xffu) | ((tag >> 8) & 0xffu),
                 ((asu(v[2]) + 0x80u) & ~0xffu) | ((tag >> 16) & 0xffu), ((asu(v[3]) + 0x80u) & ~0xffu) | (tag >> 24)};
}
__device__ __forceinline__ float v24(u32 v) { return asf(v & ~0xffu); }
__device__ __forceinline__ bool none(u32 v) { PIN(v); return __builtin_amdgcn_ballot_w64(v != 0) == 0; }
template <int N> __device__ __forceinline__ void waitvm() { asm volatile("s_waitcnt vmcnt(%0)" :: "n"(N) : "memory"); }
__device__ __forceinline__ void dma4(rsrc_t r, u32 v0, u32 v1, u32 v2, u32 v3, u32 soff, u32 ldsb) {
    asm volatile("s_mov_b32 m0, %6\n\ts_nop 0\n\t"
                 "buffer_load_dwordx4 %0, %4, %5 offen nt lds\n\t"
                 "buffer_load_dwordx4 %1, %4, %5 offen offset:1024 nt lds\n\t"
                 "buffer_load_dwordx4 %2, %4, %5 offen offset:2048 nt lds\n\t"
                 "buffer_load_dwordx4 %3, %4, %5 offen offset:3072 nt lds"
                 :: "v"(v0), "v"(v1), "v"(v2), "v"(v3), "s"(r), "s"(soff), "s"(ldsb) : "memory");
}
__device__ __forceinline__ u32x4 trfrag(LAS char* p, u32 rstep) {
    const u32x2 lo = __builtin_bit_cast(u32x2, __builtin_amdgcn_ds_read_tr16_b64_v4i16((LAS s16x4*)p));
    const u32x2 hi = __builtin_bit_cast(u32x2, __builtin_amdgcn_ds_read_tr16_b64_v4i16((LAS s16x4*)(p + rstep)));
    return u32x4{lo[0], lo[1], hi[0], hi[1]};
}
#define POLL(COND, RELOAD) u32 spins = 0; if (__builtin_expect(!(COND), 0)) { do { asm volatile("" ::: "memory"); RELOAD; ++spins; } while (!(COND) && spins < SPIN_CAP); }


__global__ void __launch_bounds__(320, 2) fused_kernel(const void*, void*, const void*, const void*, void*, const void*, const void*, const void*, const void*, const void*) {
    __shared__ __attribute__((aligned(1024))) char lds_[L_END];
    LAS char* lds = (LAS char*)lds_;
#define LD(T, off) (*(LAS T*)(lds + (off)))
#define FLAG(off) __builtin_amdgcn_readfirstlane(*(volatile LAS u32*)(lds + (off)))
#define SETFLAG(off, v) (*(volatile LAS u32*)(lds + (off)) = (v))
#define LWAIT(COND) do { u32 sp_ = 0; while (!(COND) && ++sp_ < (SPIN_CAP << 3)) __builtin_amdgcn_s_sleep(1); asm volatile("" ::: "memory"); } while (0)
#define LSIGNAL(off) do { asm volatile("s_waitcnt lgkmcnt(0)" ::: "memory"); if (l == 0) __hip_atomic_fetch_add((LAS u32*)(lds + (off)), 1u, __ATOMIC_RELAXED, __HIP_MEMORY_SCOPE_WORKGROUP); } while (0)
    u32 k0, k1, k2;
    asm volatile("s_load_dword %0, %3, 0x0\n\ts_load_dword %1, %3, 0x40\n\ts_load_dword %2, %3, 0x4c" : "=&s"(k0), "=&s"(k1), "=&s"(k2) : "s"(__builtin_amdgcn_kernarg_segment_ptr()));
    SB;
    const u32 b = blockIdx.x, h = b & 7, j = b >> 3;
    const u32 l = threadIdx.x & 63, w = __builtin_amdgcn_readfirstlane(threadIdx.x >> 6);
    const bool odd = j & 1;
    const u32 cgf = h + 8 * (j >> 3), rgf = (j >> 1) & 3;
    const bool ffn = cgf < 28;
    u64x4 A;
    u64 a_out, a_w;
#define LOAD_ARGS(WOFF, ...) asm volatile("s_load_dwordx8 %0, %3, 0x0\n\ts_load_dwordx2 %2, %3, %4\n\ts_load_dwordx2 %1, %3, 0x20\n\ts_waitcnt lgkmcnt(0)" \
                 : "=&s"(A), "=&s"(a_out), "=&s"(a_w) : "s"(__builtin_amdgcn_kernarg_segment_ptr()), "s"(WOFF), "s"(k0), "s"(k1), "s"(k2), __VA_ARGS__)

    if (w == 0) {
        const u32 kvoff = (h * SK + (SK - WIN_KEYS) + j * 16) * (DH * 2);
        const u32 w2off = (odd ? cgf * 64 + rgf * 16 : h * 512 + j * 8) * (D * 2);
        const u32 w2bytes = odd ? (ffn ? 16 * D * 2 : 0) : 272 * D * 2;
        const u32 hb = l >> 5;
        const u32 vk = hb * 512 + (((l & 31) ^ hb) << 4), vv = hb * 512 + (((l & 31) ^ (hb << 1)) << 4);
        const u32 lb = (u32)(uintptr_t)lds;
        LD(u32, L_FLAG + (l & 15) * 4) = 0u;
        LOAD_ARGS(0x28 + (j & 1) * 8, "v"(vk), "v"(vv), "s"(kvoff), "s"(w2off), "s"(w2bytes));
        const rsrc_t rk = mk(A[2] + kvoff, 0x1c2000), rv = mk(A[3] + kvoff, 0x1c2000), r2 = mk(a_w + w2off, w2bytes);
        bar();
        u32 due;
        LWAIT((due = FLAG(F_TB)) != 0);
#define PACE(T) do { while ((int)((now() << 4) - due) < 0) ; due += (T); } while (0)
#pragma unroll 1
        for (u32 g = 0; g < 8; ++g) {
            const u32 so = ((g + j) & 7) << 18;
            PACE(DT);
            dma4(rk, vk, vk ^ 32, vk ^ 64, vk ^ 96, so, lb + L_K + g * 8192);
            waitvm<WIN>();
            PACE(DT);
            dma4(rk, vk ^ 128, vk ^ 160, vk ^ 192, vk ^ 224, so + 4096, lb + L_K + g * 8192 + 4096);
            waitvm<WIN>();
        }
#pragma unroll 1
        for (u32 g = 0; g < 8; ++g) {
            const u32 so = ((g + j) & 7) << 18;
            PACE(DT);
            dma4(rv, vv, vv ^ 64, vv, vv ^ 64, so, lb + L_V + g * 8192);
            waitvm<WIN>();
            PACE(DT);
            dma4(rv, vv ^ 128, vv ^ 192, vv ^ 128, vv ^ 192, so + 4096, lb + L_V + g * 8192 + 4096);
            if (g == 7) break;
            if (g & 1) waitvm<WIN>();
            else { waitvm<24>(); SETFLAG(F_PROG, g * 8 + 48); }
        }
#define W2G(g) do { const u32 vo = l * 16 + (g) * 4096 + ((g) > 3 ? 240 * D * 2 : 0), vp = l * 16 + (g) * 4096 + ((g) > 2 ? 240 * D * 2 : 0); \
                    dma4(r2, vo, vo, vp, vp, 0, lb + L_W2 + (g) * 4096); } while (0)
        const u32 dtw = odd ? DT_WD : DT;
        { const u32 te = now(); SETFLAG(F_TE, te); due = (te << 4) + dtw; }
        PACE(dtw); W2G(0);
        PACE(dtw); W2G(1);
        waitvm<24>(); SETFLAG(F_PROG, 112);
        PACE(dtw); W2G(2);
        PACE(dtw); W2G(3);
        waitvm<16>();
        bar();
        W2G(4); W2G(5); W2G(6);
        waitvm<0>();
        SETFLAG(F_PROG, 156);
        return;
    }

    const u32 c = w - 1, g = l >> 4;
    u32 re = (j >> 4) * 4 + c, ro = rgf * 2 + (c & 1);
    asm volatile("" : "+s"(re), "+s"(ro));
    const u32 row0 = (odd ? ro : re) * 56;
    const u32 wstride = odd ? F * 2 : 8192;
    const u32 voff = (row0 + 2 * (l >> 3)) * wstride + (odd ? cgf : h * 8 + ((j >> 1) & 7)) * 128 + (l & 7) * 16;
    const u32 voff3 = l < 32 ? voff : OOR;
    const u32 w1bytes = odd ? (ffn ? D * F * 2 : 0) : D * 4096 * 2;
    const u32x4 cw = ld16<0>(mk(__builtin_amdgcn_s_getpc() & ~(u64)255, c == 3 ? CODE_KB * 1024 : 0), j * 1024 + l * 16, 0);
    LOAD_ARGS(odd ? 0x40 + (c >> 1) * 8 : 0x38, "v"(voff), "v"(voff3), "s"(w1bytes), "s"(wstride));
    const rsrc_t rws = mk(A[1], WS_END);
    const u32 hblk = h * WSBLK;
    u32 ep = 0;
#define EPOCH (*(const __attribute__((address_space(4))) u32*)A[1])
    u32 tb = now();
    const u32 ci = (j >> 1) * 8 + h;
    u32 tag, xo, ocol;
    SB;
#define UNTIL(T) while ((int)(now() - tb) < (T)) __builtin_amdgcn_s_sleep(1)
    {
        u32x4 W[8];
        const u32x4 xr = ld16<0>(mk(A[0], D * 2), l * 16, 0);
        SB;
        const rsrc_t rw = mk(a_w, w1bytes);
#pragma unroll
        for (int i = 0; i < 8; ++i) { W[i] = ld16<AUX_NT>(rw, i < 6 ? voff : voff3, ((i >> 1) * 16 + (i & 1)) * wstride); SB; }
        if (c != 3) ep = EPOCH;
        asm volatile("s_barrier" ::: "memory"); SB;
        if (c == 3) { SETFLAG(F_TB, ((tb + PACE0) << 4) | 1u); SB; ep = EPOCH; }
        ocol = (ci >> 4) * 128 + (ci & 15) * 4 + ((l >> 1) & 3) + (l & 1) * (ci >= 48 ? 32 : 64);
        xo = __builtin_amdgcn_raw_buffer_load_b16(mk(A[0], D * 2), ocol * 2, 0, 0);
        SB;
        asm volatile("" :: "v"(cw));
        float xv[8];
#pragma unroll
        for (int e = 0; e < 4; ++e) { xv[2 * e] = blo(xr[e]); xv[2 * e + 1] = bhi(xr[e]); }
        float s = 0.f;
#pragma unroll
        for (int e = 0; e < 8; ++e) s += xv[e];
        const float mean = sum_all(s) * (1.f / D);
        float q = 0.f;
#pragma unroll
        for (int e = 0; e < 8; ++e) { const float d = xv[e] - mean; q += d * d; }
        q = l < 56 ? q : 0.f;
        const float var = rbf(sum_all(q) * (1.f / D));
        const float mb = rbf(mean);
        const float rd = 1.f / rbf(__builtin_amdgcn_sqrtf(rbf(var + 1e-5f)));
        u32x4 xn;
#pragma unroll
        for (int e = 0; e < 4; ++e) xn[e] = pk(rbf(xv[2 * e] - mb) * rd, rbf(xv[2 * e + 1] - mb) * rd);
        if (l >= 56) xn = u32x4{0u, 0u, 0u, 0u};
        LD(u32x4, L_XN + l * 16) = xn;
        asm volatile("" ::: "memory");
        float a[8];
#pragma unroll
        for (int e = 0; e < 8; ++e) a[e] = 0.f;
#pragma unroll
        for (int i = 0; i < 4; ++i) {
            const u32 xp = LD(u32, L_XN + (row0 + 16 * i + 2 * (l >> 3)) * 2);
#pragma unroll
            for (int e = 0; e < 4; ++e) {
                a[2 * e] = dot2(__builtin_amdgcn_perm(W[2 * i + 1][e], W[2 * i][e], 0x05040100u), xp, a[2 * e]);
                a[2 * e + 1] = dot2(__builtin_amdgcn_perm(W[2 * i + 1][e], W[2 * i][e], 0x07060302u), xp, a[2 * e + 1]);
            }
        }
#pragma unroll
        for (int e = 0; e < 4; ++e) a[e] = tr32(a[e], a[e + 4]);
        a[0] = tr16(a[0], a[2]); a[1] = tr16(a[1], a[3]);
        a[0] += dpp<ROR8>(a[0]); a[1] += dpp<ROR8>(a[1]);
        LD(f32x2, L_Q + c * 256 + (l & 7) * 32 + g * 8) = f32x2{a[0], a[1]};
        LSIGNAL(F_C1);
        tag = ((ep & 0x7f7f7f7fu) + 0x01010101u) ^ (ep & 0x80808080u);
        if (c == 0) {
            u32 so = odd ? (((j >> 3) * 4 + ((l & 15) >> 2)) * 4 + rgf) * 128 + (l & 16) * 4 + (l & 3) * 16 + WS_GU : (j >> 4) * 2048 + ((j >> 1) & 7) * 256 + l * 16 + WS_QP;
            so = l >= (odd ? (ffn ? 32u : 0u) : 16u) ? OOR : so;
            PIN(so);
            LWAIT(FLAG(F_C1) == 4);
            f32x4 v = LD(f32x4, L_Q + (l & 16) * 32 + (l & 15) * 16) + LD(f32x4, L_Q + (l & 16) * 32 + 256 + (l & 15) * 16);
            if (!odd) {
#pragma unroll
                for (int e = 0; e < 4; ++e) v[e] = tr16(v[e], v[e]);
            }
            st16(rws, enc4(v, tag), so, hblk);
            SETFLAG(F_GATE, (now() + (odd ? 0 : QWAIT)) | 1u);
        }
    }
    { u32 gate; LWAIT((gate = FLAG(F_GATE)) != 0); LWAIT((int)(now() - gate) >= 0); UNTIL(QX); }
    u32x4 gu;
    const bool hw = odd && ffn && c == 1;
    {
        u32x4 qg;
#define QLOAD qg = ld16<AUX_SC1>(rws, (l >> 5) * 2048 + c * 512 + (l & 31) * 16, hblk + WS_QP)
#define GLOAD gu = ld16<AUX_SC1>(rws, (l & 31) * 16, hblk + WS_GU + ((j >> 3) * 4 + rgf) * 512)
        QLOAD;
        {
            POLL(none(bad4(qg, tag)), QLOAD)
        }
        LD(u32, L_Q + c * 256 + (l & 31) * 8 + (l >> 5) * 4) = pk(tr32(v24(qg[0]), v24(qg[2])), tr32(v24(qg[1]), v24(qg[3])));
        LSIGNAL(F_CQ);
        if (hw) { GLOAD; }
        const u32 r = l & 15;
        u32x4 kf[16], qa[8];
        LWAIT(FLAG(F_PROG) >= 64);
#pragma unroll
        for (int s = 0; s < 8; ++s) {
            const u32 ko = L_K + (c * 32 + r) * 512 + (((4 * (s & 3) + g) ^ r) << 4) + (s >> 2) * 256;
            kf[2 * s] = LD(u32x4, ko); kf[2 * s + 1] = LD(u32x4, ko + 8192);
        }
        SB;
        LWAIT(FLAG(F_CQ) == 4);
#pragma unroll
        for (int s = 0; s < 8; ++s) qa[s] = LD(u32x4, L_Q + (l & 1) * 512 + s * 64 + g * 16);
        SB;
        f32x4 d0 = {0.f, 0.f, 0.f, 0.f}, d1 = d0;
#pragma unroll
        for (int s = 0; s < 8; ++s) { d0 = mfma(qa[s], kf[2 * s], d0); d1 = mfma(qa[s], kf[2 * s + 1], d1); }
        const float sv = rbf(sel4(g, d0[0], d1[0], d0[1], d1[1]));
        float m = fmx(sv, dpp<XOR1>(sv));
        m = fmx(m, dpp<XOR2>(m)); m = fmx(m, dpp<ROR4>(m)); m = fmx(m, dpp<ROR8>(m)); m = max16(m);
        const __bf16 p = (__bf16)__builtin_amdgcn_exp2f((sv - m) * SCL);
        LD(__bf16, L_P + (l >> 5) * 256 + c * 64 + (l & 31) * 2) = p;
        float ls = (float)p;
        ls += dpp<XOR1>(ls); ls += dpp<XOR2>(ls); ls += dpp<ROR4>(ls); ls += dpp<ROR8>(ls); ls = tr16(ls, ls);
        if ((l & 31) == 0) { LD(float, L_ML + (l >> 5) * 16 + c * 4) = m; LD(float, L_ML + 32 + (l >> 5) * 16 + c * 4) = ls; }
        LSIGNAL(F_CS);
    }
    LWAIT(FLAG(F_CS) == 4);
    const bool cw0 = !odd && c == 0;
    u32x4 sg[4];
#define SLOAD _Pragma("unroll") for (int i = 0; i < 4; ++i) sg[i] = ld16<AUX_SC1>(rws, (l >> 3) * 16, hblk + WS_ST + i * 128)
    {
        u32x4 PA[4];
        f32x4 f0, f1;
#pragma unroll
        for (int s = 0; s < 4; ++s) PA[s] = LD(u32x4, L_P + (l & 1) * 256 + s * 64 + g * 16);
        const f32x4 m0 = LD(f32x4, L_ML), m1 = LD(f32x4, L_ML + 16), l0 = LD(f32x4, L_ML + 32), l1 = LD(f32x4, L_ML + 48);
        const float M0 = fmx(fmx(m0[0], m0[1]), fmx(m0[2], m0[3])), M1 = fmx(fmx(m1[0], m1[1]), fmx(m1[2], m1[3]));
#pragma unroll
        for (int s = 0; s < 4; ++s) { f0[s] = __builtin_amdgcn_exp2f((m0[s] - M0) * SCL); f1[s] = __builtin_amdgcn_exp2f((m1[s] - M1) * SCL); }
        if (c == 3)
            st16(rws, enc4(f32x4{M0, f0[0] * l0[0] + f0[1] * l0[1] + f0[2] * l0[2] + f0[3] * l0[3], M1, f1[0] * l1[0] + f1[1] * l1[1] + f1[2] * l1[2] + f1[3] * l1[3]}, tag),
                 l == 0 ? j * 16 : OOR, hblk + WS_ST);
        const u32 q = (l >> 2) & 3, f = q | ((g & 1) << 2);
        u32 vo[4], pg = 0, opo = (c * 64 + l) * 8;
        PIN(opo);
        float v0 = 0.f, v1 = 0.f;
#pragma unroll
        for (int u = 0; u < 4; ++u) vo[u] = L_V + (8 * g + q) * 512 + (((4 * c + u) ^ f) << 5) + (l & 3) * 8;
#pragma unroll
        for (int s = 0; s < 4; ++s) {
            if (s < 3) { if (pg < 80 + 16 * s) LWAIT((pg = FLAG(F_PROG)) >= 80 + 16 * s); }
            else {
                if (hw) {
                    POLL(none(bad4(gu, tag)), GLOAD)
                    float hv[4];
#pragma unroll
                    for (int e = 0; e < 4; ++e) {
                        float x = v24(gu[e]);
                        x += dpp<ROR8>(x); x = rbf(tr16(x, x));
                        hv[e] = rbf(silu(x)) * dpp<ROR4>(x);
                    }
                    if (l < 8) LD(u32x2, L_A + l * 8) = l < 4 ? u32x2{pk(hv[0], hv[1]), pk(hv[2], hv[3])} : u32x2{0u, 0u};
                }
                bar();
            }
            f32x4 d[4];
#pragma unroll
            for (int u = 0; u < 4; ++u) d[u] = mfma(PA[s], trfrag(lds + vo[u] + s * 16384, 2048), f32x4{0.f, 0.f, 0.f, 0.f});
            v0 = fmaf(f0[s], sel4(g, d[0][0], d[1][0], d[2][0], d[3][0]), v0);
            v1 = fmaf(f1[s], sel4(g, d[0][1], d[1][1], d[2][1], d[3][1]), v1);
        }
        st8(rws, u32x2{pk(v0, v1), tag}, opo, hblk + WS_OP + j * 2048);
        SB;
    }
    tb = FLAG(F_TE);
    u32x4 og[4];
    f32x2 wp[4];
#define OLOAD _Pragma("unroll") for (int i = 0; i < 4; ++i) og[i] = ld16<AUX_SC1>(rws, (l >> 3) * 2048 + (l & 7) * 16, hblk + WS_OP + i * 16384 + (j >> 1) * 128)
    u32 spo, bo, ao[4], la = L_A + ((l >> 5) * 8 + (l & 7)) * 4;
    {
        const u32 cc = c * 16 + (l >> 2);
        spo = cc < 56 ? (cc & 7) * WSBLK + (cc >> 3) * SP_STRIDE + (odd ? cgf * 4 + rgf : ci) * 64 + (l & 3) * 16 : OOR;
        bo = L_W2 + (((8 * g) & (odd ? 15 : 31)) + ((l >> 2) & 3)) * 896 + c * 256 + (l & 3) * 8;
#pragma unroll
        for (int u = 0; u < 4; ++u) { ao[u] = ((l >> 2) & 3) == u ? L_A + g * 16 : L_ZERO; PIN(ao[u]); }
        PIN(spo); PIN(bo); PIN(la);
    }
    if (cw0) {
        UNTIL(SX);
        SLOAD; SB;
        { const u32 tw = now() + HWAIT; while ((int)(now() - tw) < 0) ; }
        UNTIL(HX);
        OLOAD; SB;
        POLL(none(bad4(sg[0], tag) | bad4(sg[1], tag) | bad4(sg[2], tag) | bad4(sg[3], tag)), SLOAD)
#pragma unroll
        for (int hd = 0; hd < 2; ++hd) {
            const float M = max_hi3(fmx(fmx(v24(sg[0][2 * hd]), v24(sg[1][2 * hd])), fmx(v24(sg[2][2 * hd]), v24(sg[3][2 * hd]))));
            float L = 0.f;
#pragma unroll
            for (int i = 0; i < 4; ++i) { wp[i][hd] = __builtin_amdgcn_exp2f((v24(sg[i][2 * hd]) - M) * SCL); L = fmaf(wp[i][hd], v24(sg[i][2 * hd + 1]), L); }
            const float rl = __builtin_amdgcn_rcpf(sum_hi3(L));
#pragma unroll
            for (int i = 0; i < 4; ++i) wp[i][hd] *= rl;
        }
        PIN(wp[0]); PIN(wp[1]); PIN(wp[2]); PIN(wp[3]); SB;
    }
    LWAIT(FLAG(F_PROG) == 156);
    u32x4 B2[8];
#pragma unroll
    for (int u = 0; u < 8; ++u) B2[u] = trfrag(lds + bo - (c == 3 ? ((u + 2) >> 2) * 64 : 0) + u * 32, 4 * 896);
    if (!odd) {
        if (c == 0) {
            POLL(none(bad(og[0], tag) | bad(og[1], tag) | bad(og[2], tag) | bad(og[3], tag)), OLOAD)
            f32x2 a0 = {0.f, 0.f}, a1 = a0;
#pragma unroll
            for (int i = 0; i < 4; ++i) {
                a0 = __builtin_elementwise_fma(wp[i], f32x2{blo(og[i][0]), bhi(og[i][0])}, a0);
                a1 = __builtin_elementwise_fma(wp[i], f32x2{blo(og[i][2]), bhi(og[i][2])}, a1);
            }
            float o0 = tr32(a0[0], a0[1]), o1 = tr32(a1[0], a1[1]);
            o0 = tr16(o0, o0); o1 = tr16(o1, o1);
            o0 += dpp<ROR8>(o0); o1 += dpp<ROR8>(o1);
            LD(u32, la) = pk(o0, o1);
        }
        bar();
    }
    if (!odd || ffn) {
        f32x4 da = {0.f, 0.f, 0.f, 0.f}, db = da;
        u32x4 A2[4];
#pragma unroll
        for (int u = 0; u < 4; ++u) A2[u] = LD(u32x4, ao[u]);
#pragma unroll
        for (int u = 0; u < 4; ++u) { da = mfma(A2[u], B2[u], da); db = mfma(A2[u], B2[u + 4], db); }
        st16(rws, u32x4{asu(da[0]), tag, asu(db[0]), tag}, spo, odd ? WS_FP : WS_SP);
        SB;
    }
    if (odd && ci < 56) {
        const u32 o0 = hblk + (j >> 1) * SP_STRIDE + c * 2048 + l * 16;
        const u32 o1 = c == 3 ? OOR : o0 + 1024, tg1 = c == 3 ? 0u : tag;
        u32 lf = L_FIN + ((l & 3) * 2 + (l >> 5)) * 16 + c * 4;
        PIN(lf);
        u32x4 g0, g1, g2, g3;
        float gate;
#define FLOAD g2 = ld16<AUX_SC1>(rws, o0, WS_FP); g3 = ld16<AUX_SC1>(rws, o1, WS_FP)
#define PLOAD g0 = ld16<AUX_SC1>(rws, o0, WS_SP); g1 = ld16<AUX_SC1>(rws, o0, WS_SP + 1024)
        UNTIL(GX);
        FLOAD; SB;
        {
            POLL(none(bad(g2, tag) | bad(g3, tg1)), FLOAD)
            float f = tr32(asf(g2[0]) + asf(g3[0]), asf(g2[2]) + asf(g3[2]));
            f = tr16(f, f); f += dpp<ROR8>(f); f += dpp<ROR4>(f);
            LD(float, lf + 128) = f;
            bar();
            const f32x4 r = LD(f32x4, L_FIN + 128 + (l & 7) * 16);
            gate = rbf(silu(rbf(r[0] + r[1] + r[2] + r[3])));
            PIN(gate);
        }
        __builtin_amdgcn_s_sleep(FD);
        PLOAD;
        POLL(none(bad(g0, tag) | bad(g1, tag)), PLOAD)
        float s = tr32(asf(g0[0]) + asf(g1[0]), asf(g0[2]) + asf(g1[2]));
        s = tr16(s, s); s += dpp<ROR8>(s); s += dpp<ROR4>(s);
        LD(float, lf) = s;
        bar();
        if (c == 0) {
            const f32x4 r = LD(f32x4, L_FIN + (l & 7) * 16);
            const float y = blo(xo) + rbf(rbf(r[0] + r[1] + r[2] + r[3]) * gate);
            __builtin_amdgcn_raw_buffer_store_b16(__builtin_bit_cast(u16, (__bf16)y), mk(a_out, D * 2), l < 8 ? ocol * 2 : OOR, 0, 0);
            if (ci == 0) __builtin_amdgcn_raw_buffer_store_b32(tag, rws, l == 0 ? 0u : OOR, 0, AUX_SC1);
        }
    }
}

extern "C" void kernel_launch(void* const* d_in, const int* in_sizes, int n_in, void* d_out, int out_size, void* d_ws, size_t ws_size, hipStream_t stream) {
    fused_kernel<<<256, 320, 0, stream>>>(d_in[0], d_ws, d_in[1], d_in[2], d_out, d_in[4], d_in[8], d_in[3], d_in[6], d_in[7]);
}
```

```cpp
#include <hip/hip_runtime.h>
#include <hip/hip_bf16.h>
#include <stdint.h>

typedef unsigned short u16;
typedef unsigned int u32;
typedef unsigned long long u64;
typedef u32 u32x4 __attribute__((ext_vector_type(4)));
typedef u32 u32x2 __attribute__((ext_vector_type(2)));
typedef u64 u64x4 __attribute__((ext_vector_type(4)));
typedef float f32x2 __attribute__((ext_vector_type(2)));
typedef float f32x4 __attribute__((ext_vector_type(4)));
typedef __bf16 bf16x2 __attribute__((ext_vector_type(2)));
typedef __bf16 bf16x8 __attribute__((ext_vector_type(8)));
typedef short s16x4 __attribute__((ext_vector_type(4)));
typedef __amdgpu_buffer_rsrc_t rsrc_t;
#define LAS __attribute__((address_space(3)))

constexpr int D = 448, SK = 65536, DH = 256, WIN_KEYS = 4096, F = 1792;
constexpr u32 WSBLK = 262144, SP_STRIDE = 8192;
constexpr u32 WS_QP = 256;
constexpr u32 WS_ST = WS_QP + 4096;
constexpr u32 WS_GU = WS_ST + 1024;
constexpr u32 WS_OP = WS_GU + 8192;
constexpr u32 WS_SP = WS_OP + 65536;
constexpr u32 WS_FP = WS_SP + 7 * SP_STRIDE;
constexpr u32 WS_END = 8 * WSBLK;
static_assert(WS_FP + 7 * SP_STRIDE <= WSBLK, "workspace block");
constexpr u32 L_K = 0, L_V = 65536, L_W2 = 131072, L_XN = L_W2 + 28672, L_Q = L_XN + 1024, L_P = L_Q + 1024, L_ML = L_P + 512, L_A = L_ML + 64,
              L_FIN = L_A + 64, L_FLAG = L_FIN + 256, L_END = L_FLAG + 64;
constexpr u32 F_PROG = L_FLAG, F_GATE = L_FLAG + 4, F_CQ = L_FLAG + 8, F_CS = L_FLAG + 12, F_C1 = L_FLAG + 16, F_TE = L_FLAG + 20, F_TB = L_FLAG + 24, L_ZERO = L_FLAG + 32;
constexpr int AUX_NT = 2, AUX_SC1 = 16;
constexpr float SCL = 0.0625f * 1.4426950408889634f;
constexpr u32 SPIN_CAP = 1u << 18, OOR = 0x80000000u;
constexpr int PACE0 = 100;
constexpr int DT = 232;
constexpr int DT_WD = 64;
constexpr int WIN = 36;
constexpr int QWAIT = 50;
constexpr int QX = 292;
constexpr int SX = 90;
constexpr int HWAIT = 20, HX = 150;
constexpr int GX = 210;
constexpr int FD = 8;
constexpr int CODE_KB = 12;

#define SB __builtin_amdgcn_sched_barrier(0)
#define PIN(x) asm volatile("" : "+v"(x))
__device__ __forceinline__ rsrc_t mk(u64 p, u32 bytes) { return __builtin_amdgcn_make_buffer_rsrc((void*)p, 0, bytes, 0x00020000); }
template <int AUX> __device__ __forceinline__ u32x4 ld16(rsrc_t r, u32 voff, u32 soff) { return __builtin_amdgcn_raw_buffer_load_b128(r, voff, soff, AUX); }
__device__ __forceinline__ void st16(rsrc_t r, u32x4 v, u32 voff, u32 soff) { __builtin_amdgcn_raw_buffer_store_b128(v, r, voff, soff, AUX_SC1); }
__device__ __forceinline__ void st8(rsrc_t r, u32x2 v, u32 voff, u32 soff) { __builtin_amdgcn_raw_buffer_store_b64(v, r, voff, soff, AUX_SC1); }
__device__ __forceinline__ u32 now() { return (u32)__builtin_amdgcn_s_memrealtime(); }
__device__ __forceinline__ float asf(u32 v) { return __builtin_bit_cast(float, v); }
__device__ __forceinline__ u32 asu(float v) { return __builtin_bit_cast(u32, v); }
__device__ __forceinline__ float blo(u32 v) { return asf(v << 16); }
__device__ __forceinline__ float bhi(u32 v) { return asf(v & 0xffff0000u); }
__device__ __forceinline__ float rbf(float f) { return (float)(__bf16)f; }
__device__ __forceinline__ u32 pk(float a, float b) { return __builtin_bit_cast(u32, bf16x2{(__bf16)a, (__bf16)b}); }
__device__ __forceinline__ float dot2(u32 a, u32 b, float c) { return __builtin_amdgcn_fdot2_f32_bf16(__builtin_bit_cast(bf16x2, a), __builtin_bit_cast(bf16x2, b), c, false); }
__device__ __forceinline__ f32x4 mfma(u32x4 a, u32x4 b, f32x4 c) { return __builtin_amdgcn_mfma_f32_16x16x32_bf16(__builtin_bit_cast(bf16x8, a), __builtin_bit_cast(bf16x8, b), c, 0, 0, 0); }
template <int CTRL> __device__ __forceinline__ float dpp(float x) { return asf(__builtin_amdgcn_mov_dpp((int)asu(x), CTRL, 0xf, 0xf, true)); }
constexpr int XOR1 = 0xB1, XOR2 = 0x4E, ROR8 = 0x128, ROR4 = 0x124;
__device__ __forceinline__ float tr32(float x, float y) { auto s = __builtin_amdgcn_permlane32_swap(asu(x), asu(y), false, false); return asf(s[0]) + asf(s[1]); }
__device__ __forceinline__ float tr16(float x, float y) { auto s = __builtin_amdgcn_permlane16_swap(asu(x), asu(y), false, false); return asf(s[0]) + asf(s[1]); }
__device__ __forceinline__ float fmx(float a, float b) { return __builtin_elementwise_maximum(a, b); }
__device__ __forceinline__ float max32(float x) { auto s = __builtin_amdgcn_permlane32_swap(asu(x), asu(x), false, false); return fmx(asf(s[0]), asf(s[1])); }
__device__ __forceinline__ float max16(float x) { auto s = __builtin_amdgcn_permlane16_swap(asu(x), asu(x), false, false); return fmx(asf(s[0]), asf(s[1])); }
__device__ __forceinline__ float sum_hi3(float x) { x += dpp<ROR8>(x); x = tr16(x, x); return tr32(x, x); }
__device__ __forceinline__ float sum_all(float x) { x += dpp<XOR1>(x); x += dpp<XOR2>(x); x += dpp<ROR4>(x); return sum_hi3(x); }
__device__ __forceinline__ float max_hi3(float x) { x = fmx(x, dpp<ROR8>(x)); return max32(max16(x)); }
__device__ __forceinline__ float sel4(u32 g, float a, float b, float c, float d) { return g & 2 ? (g & 1 ? d : c) : (g & 1 ? b : a); }
__device__ __forceinline__ float silu(float v) { return v * __builtin_amdgcn_rcpf(1.f + __expf(-v)); }
__device__ __forceinline__ void bar() { SB; asm volatile("s_waitcnt lgkmcnt(0)\n\ts_barrier" ::: "memory"); SB; }
__device__ __forceinline__ u32 bad(u32x4 g, u32 tag) { return (g[1] ^ tag) | (g[3] ^ tag); }
__device__ __forceinline__ u32 bad4(u32x4 g, u32 tag) {
    return __builtin_amdgcn_perm(__builtin_amdgcn_perm(g[3], g[2], 0x0c0c0400u), __builtin_amdgcn_perm(g[1], g[0], 0x0c0c0400u), 0x05040100u) ^ tag;
}
__device__ __forceinline__ u32x4 enc4(f32x4 v, u32 tag) {
    return u32x4{((asu(v[0]) + 0x80u) & ~0xffu) | (tag & 0xffu), ((asu(v[1]) + 0x80u) & ~0xffu) | ((tag >> 8) & 0xffu),
                 ((asu(v[2]) + 0x80u) & ~0xffu) | ((tag >> 16) & 0xffu), ((asu(v[3]) + 0x80u) & ~0xffu) | (tag >> 24)};
}
__device__ __forceinline__ float v24(u32 v) { return asf(v & ~0xffu); }
__device__ __forceinline__ bool none(u32 v) { PIN(v); return __builtin_amdgcn_ballot_w64(v != 0) == 0; }
template <int N> __device__ __forceinline__ void waitvm() { asm volatile("s_waitcnt vmcnt(%0)" :: "n"(N) : "memory"); }
__device__ __forceinline__ void dma4(rsrc_t r, u32 v0, u32 v1, u32 v2, u32 v3, u32 soff, u32 ldsb) {
    asm volatile("s_mov_b32 m0, %6\n\ts_nop 0\n\t"
                 "buffer_load_dwordx4 %0, %4, %5 offen nt lds\n\t"
                 "buffer_load_dwordx4 %1, %4, %5 offen offset:1024 nt lds\n\t"
                 "buffer_load_dwordx4 %2, %4, %5 offen offset:2048 nt lds\n\t"
                 "buffer_load_dwordx4 %3, %4, %5 offen offset:3072 nt lds"
                 :: "v"(v0), "v"(v1), "v"(v2), "v"(v3), "s"(r), "s"(soff), "s"(ldsb) : "memory");
}
__device__ __forceinline__ u32x4 trfrag(LAS char* p, u32 rstep) {
    const u32x2 lo = __builtin_bit_cast(u32x2, __builtin_amdgcn_ds_read_tr16_b64_v4i16((LAS s16x4*)p));
    const u32x2 hi = __builtin_bit_cast(u32x2, __builtin_amdgcn_ds_read_tr16_b64_v4i16((LAS s16x4*)(p + rstep)));
    return u32x4{lo[0], lo[1], hi[0], hi[1]};
}
#define POLL(COND, RELOAD) u32 spins = 0; if (__builtin_expect(!(COND), 0)) { do { asm volatile("" ::: "memory"); RELOAD; ++spins; } while (!(COND) && spins < SPIN_CAP); }


__global__ void __launch_bounds__(320, 2) fused_kernel(const void*, void*, const void*, const void*, void*, const void*, const void*, const void*, const void*, const void*) {
    __shared__ __attribute__((aligned(1024))) char lds_[L_END];
    LAS char* lds = (LAS char*)lds_;
#define LD(T, off) (*(LAS T*)(lds + (off)))
#define FLAG(off) __builtin_amdgcn_readfirstlane(*(volatile LAS u32*)(lds + (off)))
#define SETFLAG(off, v) (*(volatile LAS u32*)(lds + (off)) = (v))
#define LWAIT(COND) do { u32 sp_ = 0; while (!(COND) && ++sp_ < (SPIN_CAP << 3)) __builtin_amdgcn_s_sleep(1); asm volatile("" ::: "memory"); } while (0)
#define LSIGNAL(off) do { asm volatile("s_waitcnt lgkmcnt(0)" ::: "memory"); if (l == 0) __hip_atomic_fetch_add((LAS u32*)(lds + (off)), 1u, __ATOMIC_RELAXED, __HIP_MEMORY_SCOPE_WORKGROUP); } while (0)
    u32 k0, k1, k2;
    asm volatile("s_load_dword %0, %3, 0x0\n\ts_load_dword %1, %3, 0x40\n\ts_load_dword %2, %3, 0x4c" : "=&s"(k0), "=&s"(k1), "=&s"(k2) : "s"(__builtin_amdgcn_kernarg_segment_ptr()));
    SB;
    const u32 b = blockIdx.x, h = b & 7, j = b >> 3;
    const u32 l = threadIdx.x & 63, w = __builtin_amdgcn_readfirstlane(threadIdx.x >> 6);
    const bool odd = j & 1;
    const u32 cgf = h + 8 * (j >> 3), rgf = (j >> 1) & 3;
    const bool ffn = cgf < 28;
    u64x4 A;
    u64 a_out, a_w;
#define LOAD_ARGS(WOFF, ...) asm volatile("s_load_dwordx8 %0, %3, 0x0\n\ts_load_dwordx2 %2, %3, %4\n\ts_load_dwordx2 %1, %3, 0x20\n\ts_waitcnt lgkmcnt(0)" \
                 : "=&s"(A), "=&s"(a_out), "=&s"(a_w) : "s"(__builtin_amdgcn_kernarg_segment_ptr()), "s"(WOFF), "s"(k0), "s"(k1), "s"(k2), __VA_ARGS__)

    if (w == 0) {
        const u32 kvoff = (h * SK + (SK - WIN_KEYS) + j * 16) * (DH * 2);
        const u32 w2off = (odd ? cgf * 64 + rgf * 16 : h * 512 + j * 8) * (D * 2);
        const u32 w2bytes = odd ? (ffn ? 16 * D * 2 : 0) : 272 * D * 2;
        const u32 hb = l >> 5;
        const u32 vk = hb * 512 + (((l & 31) ^ hb) << 4), vv = hb * 512 + (((l & 31) ^ (hb << 1)) << 4);
        const u32 lb = (u32)(uintptr_t)lds;
        LD(u32, L_FLAG + (l & 15) * 4) = 0u;
        LOAD_ARGS(0x28 + (j & 1) * 8, "v"(vk), "v"(vv), "s"(kvoff), "s"(w2off), "s"(w2bytes));
        const rsrc_t rk = mk(A[2] + kvoff, 0x1c2000), rv = mk(A[3] + kvoff, 0x1c2000), r2 = mk(a_w + w2off, w2bytes);
        bar();
        u32 due;
        LWAIT((due = FLAG(F_TB)) != 0);
#define PACE(T) do { while ((int)((now() << 4) - due) < 0) ; due += (T); } while (0)
#pragma unroll 1
        for (u32 g = 0; g < 8; ++g) {
            const u32 so = ((g + j) & 7) << 18;
            PACE(DT);
            dma4(rk, vk, vk ^ 32, vk ^ 64, vk ^ 96, so, lb + L_K + g * 8192);
            waitvm<WIN>();
            PACE(DT);
            dma4(rk, vk ^ 128, vk ^ 160, vk ^ 192, vk ^ 224, so + 4096, lb + L_K + g * 8192 + 4096);
            waitvm<WIN>();
        }
#pragma unroll 1
        for (u32 g = 0; g < 8; ++g) {
            const u32 so = ((g + j) & 7) << 18;
            PACE(DT);
            dma4(rv, vv, vv ^ 64, vv, vv ^ 64, so, lb + L_V + g * 8192);
            waitvm<WIN>();
            PACE(DT);
            dma4(rv, vv ^ 128, vv ^ 192, vv ^ 128, vv ^ 192, so + 4096, lb + L_V + g * 8192 + 4096);
            if (g == 7) break;
            if (g & 1) waitvm<WIN>();
            else { waitvm<24>(); SETFLAG(F_PROG, g * 8 + 48); }
        }
#define W2G(g) do { const u32 vo = l * 16 + (g) * 4096 + ((g) > 3 ? 240 * D * 2 : 0), vp = l * 16 + (g) * 4096 + ((g) > 2 ? 240 * D * 2 : 0); \
                    dma4(r2, vo, vo, vp, vp, 0, lb + L_W2 + (g) * 4096); } while (0)
        const u32 dtw = odd ? DT_WD : DT;
        { const u32 te = now(); SETFLAG(F_TE, te); due = (te << 4) + dtw; }
        PACE(dtw); W2G(0);
        PACE(dtw); W2G(1);
        waitvm<24>(); SETFLAG(F_PROG, 112);
        PACE(dtw); W2G(2);
        PACE(dtw); W2G(3);
        waitvm<16>();
        bar();
        W2G(4); W2G(5); W2G(6);
        waitvm<0>();
        SETFLAG(F_PROG, 156);
        return;
    }

    const u32 c = w - 1, g = l >> 4;
    u32 re = (j >> 4) * 4 + c, ro = rgf * 2 + (c & 1);
    asm volatile("" : "+s"(re), "+s"(ro));
    const u32 row0 = (odd ? ro : re) * 56;
    const u32 wstride = odd ? F * 2 : 8192;
    const u32 voff = (row0 + 2 * (l >> 3)) * wstride + (odd ? cgf : h * 8 + ((j >> 1) & 7)) * 128 + (l & 7) * 16;
    const u32 voff3 = l < 32 ? voff : OOR;
    const u32 w1bytes = odd ? (ffn ? D * F * 2 : 0) : D * 4096 * 2;
    const u32x4 cw = ld16<0>(mk(__builtin_amdgcn_s_getpc() & ~(u64)255, c == 3 ? CODE_KB * 1024 : 0), j * 1024 + l * 16, 0);
    LOAD_ARGS(odd ? 0x40 + (c >> 1) * 8 : 0x38, "v"(voff), "v"(voff3), "s"(w1bytes), "s"(wstride));
    const rsrc_t rws = mk(A[1], WS_END);
    const u32 hblk = h * WSBLK;
    u32 ep = 0;
#define EPOCH (*(const __attribute__((address_space(4))) u32*)A[1])
    u32 tb = now();
    const u32 fc = h * 16 + (j >> 1);
    u32 tag, xo, ocol;
    SB;
#define UNTIL(T) while ((int)(now() - tb) < (T)) __builtin_amdgcn_s_sleep(1)
    {
        u32x4 W[8];
        const u32x4 xr = ld16<0>(mk(A[0], D * 2), l * 16, 0);
        SB;
        const rsrc_t rw = mk(a_w, w1bytes);
#pragma unroll
        for (int i = 0; i < 8; ++i) { W[i] = ld16<AUX_NT>(rw, i < 6 ? voff : voff3, ((i >> 1) * 16 + (i & 1)) * wstride); SB; }
        if (c != 3) ep = EPOCH;
        asm volatile("s_barrier" ::: "memory"); SB;
        if (c == 3) { SETFLAG(F_TB, ((tb + PACE0) << 4) | 1u); SB; ep = EPOCH; }
        ocol = (fc >> 4) * 128 + (fc & 15) * 4 + ((l >> 1) & 3) + (l & 1) * (fc >= 48 ? 32 : 64);
        xo = __builtin_amdgcn_raw_buffer_load_b16(mk(A[0], D * 2), ocol * 2, 0, 0);
        SB;
        asm volatile("" :: "v"(cw));
        float xv[8];
#pragma unroll
        for (int e = 0; e < 4; ++e) { xv[2 * e] = blo(xr[e]); xv[2 * e + 1] = bhi(xr[e]); }
        float s = 0.f;
#pragma unroll
        for (int e = 0; e < 8; ++e) s += xv[e];
        const float mean = sum_all(s) * (1.f / D);
        float q = 0.f;
#pragma unroll
        for (int e = 0; e < 8; ++e) { const float d = xv[e] - mean; q += d * d; }
        q = l < 56 ? q : 0.f;
        const float var = rbf(sum_all(q) * (1.f / D));
        const float mb = rbf(mean);
        const float rd = 1.f / rbf(__builtin_amdgcn_sqrtf(rbf(var + 1e-5f)));
        u32x4 xn;
#pragma unroll
        for (int e = 0; e < 4; ++e) xn[e] = pk(rbf(xv[2 * e] - mb) * rd, rbf(xv[2 * e + 1] - mb) * rd);
        if (l >= 56) xn = u32x4{0u, 0u, 0u, 0u};
        LD(u32x4, L_XN + l * 16) = xn;
        asm volatile("" ::: "memory");
        float a[8];
#pragma unroll
        for (int e = 0; e < 8; ++e) a[e] = 0.f;
#pragma unroll
        for (int i = 0; i < 4; ++i) {
            const u32 xp = LD(u32, L_XN + (row0 + 16 * i + 2 * (l >> 3)) * 2);
#pragma unroll
            for (int e = 0; e < 4; ++e) {
                a[2 * e] = dot2(__builtin_amdgcn_perm(W[2 * i + 1][e], W[2 * i][e], 0x05040100u), xp, a[2 * e]);
                a[2 * e + 1] = dot2(__builtin_amdgcn_perm(W[2 * i + 1][e], W[2 * i][e], 0x07060302u), xp, a[2 * e + 1]);
            }
        }
#pragma unroll
        for (int e = 0; e < 4; ++e) a[e] = tr32(a[e], a[e + 4]);
        a[0] = tr16(a[0], a[2]); a[1] = tr16(a[1], a[3]);
        a[0] += dpp<ROR8>(a[0]); a[1] += dpp<ROR8>(a[1]);
        LD(f32x2, L_Q + c * 256 + (l & 7) * 32 + g * 8) = f32x2{a[0], a[1]};
        tag = ((ep & 0x7f7f7f7fu) + 0x01010101u) ^ (ep & 0x80808080u);
        asm volatile("" : "+s"(tag));
        LSIGNAL(F_C1);
        if (c == 0) {
            u32 so = odd ? (((j >> 3) * 4 + ((l & 15) >> 2)) * 4 + rgf) * 128 + (l & 16) * 4 + (l & 3) * 16 + WS_GU : (j >> 4) * 2048 + ((j >> 1) & 7) * 256 + l * 16 + WS_QP;
            so = l >= (odd ? (ffn ? 32u : 0u) : 16u) ? OOR : so;
            PIN(so);
            LWAIT(FLAG(F_C1) == 4);
            f32x4 v = LD(f32x4, L_Q + (l & 16) * 32 + (l & 15) * 16) + LD(f32x4, L_Q + (l & 16) * 32 + 256 + (l & 15) * 16);
            if (!odd) {
#pragma unroll
                for (int e = 0; e < 4; ++e) v[e] = tr16(v[e], v[e]);
            }
            st16(rws, enc4(v, tag), so, hblk);
            SETFLAG(F_GATE, (now() + (odd ? 0 : QWAIT)) | 1u);
        }
    }
    { u32 gate; LWAIT((gate = FLAG(F_GATE)) != 0); LWAIT((int)(now() - gate) >= 0); UNTIL(QX); }
    u32x4 gu;
    const bool hw = odd && ffn && c == 1;
    {
        u32x4 qg;
#define QLOAD qg = ld16<AUX_SC1>(rws, (l >> 5) * 2048 + c * 512 + (l & 31) * 16, hblk + WS_QP)
#define GLOAD gu = ld16<AUX_SC1>(rws, (l & 31) * 16, hblk + WS_GU + ((j >> 3) * 4 + rgf) * 512)
        QLOAD;
        {
            POLL(none(bad4(qg, tag)), QLOAD)
        }
        LD(u32, L_Q + c * 256 + (l & 31) * 8 + (l >> 5) * 4) = pk(tr32(v24(qg[0]), v24(qg[2])), tr32(v24(qg[1]), v24(qg[3])));
        LSIGNAL(F_CQ);
        if (hw) { GLOAD; }
        const u32 r = l & 15;
        u32x4 kf[16], qa[8];
        LWAIT(FLAG(F_PROG) >= 64);
#pragma unroll
        for (int s = 0; s < 8; ++s) {
            const u32 ko = L_K + (c * 32 + r) * 512 + (((4 * (s & 3) + g) ^ r) << 4) + (s >> 2) * 256;
            kf[2 * s] = LD(u32x4, ko); kf[2 * s + 1] = LD(u32x4, ko + 8192);
        }
        SB;
        LWAIT(FLAG(F_CQ) == 4);
#pragma unroll
        for (int s = 0; s < 8; ++s) qa[s] = LD(u32x4, L_Q + (l & 1) * 512 + s * 64 + g * 16);
        SB;
        f32x4 d0 = {0.f, 0.f, 0.f, 0.f}, d1 = d0;
#pragma unroll
        for (int s = 0; s < 8; ++s) { d0 = mfma(qa[s], kf[2 * s], d0); d1 = mfma(qa[s], kf[2 * s + 1], d1); }
        const float sv = rbf(sel4(g, d0[0], d1[0], d0[1], d1[1]));
        float m = fmx(sv, dpp<XOR1>(sv));
        m = fmx(m, dpp<XOR2>(m)); m = fmx(m, dpp<ROR4>(m)); m = fmx(m, dpp<ROR8>(m)); m = max16(m);
        const __bf16 p = (__bf16)__builtin_amdgcn_exp2f((sv - m) * SCL);
        LD(__bf16, L_P + (l >> 5) * 256 + c * 64 + (l & 31) * 2) = p;
        float ls = (float)p;
        ls += dpp<XOR1>(ls); ls += dpp<XOR2>(ls); ls += dpp<ROR4>(ls); ls += dpp<ROR8>(ls); ls = tr16(ls, ls);
        if ((l & 31) == 0) { LD(float, L_ML + (l >> 5) * 16 + c * 4) = m; LD(float, L_ML + 32 + (l >> 5) * 16 + c * 4) = ls; }
        LSIGNAL(F_CS);
    }
    LWAIT(FLAG(F_CS) == 4);
    const bool cw0 = !odd && c == 0;
    u32x4 sg[4];
#define SLOAD _Pragma("unroll") for (int i = 0; i < 4; ++i) sg[i] = ld16<AUX_SC1>(rws, (l >> 3) * 16, hblk + WS_ST + i * 128)
    {
        u32x4 PA[4];
        f32x4 f0, f1;
#pragma unroll
        for (int s = 0; s < 4; ++s) PA[s] = LD(u32x4, L_P + (l & 1) * 256 + s * 64 + g * 16);
        const f32x4 m0 = LD(f32x4, L_ML), m1 = LD(f32x4, L_ML + 16), l0 = LD(f32x4, L_ML + 32), l1 = LD(f32x4, L_ML + 48);
        const float M0 = fmx(fmx(m0[0], m0[1]), fmx(m0[2], m0[3])), M1 = fmx(fmx(m1[0], m1[1]), fmx(m1[2], m1[3]));
#pragma unroll
        for (int s = 0; s < 4; ++s) { f0[s] = __builtin_amdgcn_exp2f((m0[s] - M0) * SCL); f1[s] = __builtin_amdgcn_exp2f((m1[s] - M1) * SCL); }
        if (c == 3)
            st16(rws, enc4(f32x4{M0, f0[0] * l0[0] + f0[1] * l0[1] + f0[2] * l0[2] + f0[3] * l0[3], M1, f1[0] * l1[0] + f1[1] * l1[1] + f1[2] * l1[2] + f1[3] * l1[3]}, tag),
                 l == 0 ? j * 16 : OOR, hblk + WS_ST);
        const u32 q = (l >> 2) & 3, f = q | ((g & 1) << 2);
        u32 vo[4], pg = 0, opo = (c * 64 + l) * 8;
        PIN(opo);
        float v0 = 0.f, v1 = 0.f;
#pragma unroll
        for (int u = 0; u < 4; ++u) vo[u] = L_V + (8 * g + q) * 512 + (((4 * c + u) ^ f) << 5) + (l & 3) * 8;
#pragma unroll
        for (int s = 0; s < 4; ++s) {
            if (s < 3) { if (pg < 80 + 16 * s) LWAIT((pg = FLAG(F_PROG)) >= 80 + 16 * s); }
            else {
                if (hw) {
                    POLL(none(bad4(gu, tag)), GLOAD)
                    float hv[4];
#pragma unroll
                    for (int e = 0; e < 4; ++e) {
                        float x = v24(gu[e]);
                        x += dpp<ROR8>(x); x = rbf(tr16(x, x));
                        hv[e] = rbf(silu(x)) * dpp<ROR4>(x);
                    }
                    if (l < 8) LD(u32x2, L_A + l * 8) = l < 4 ? u32x2{pk(hv[0], hv[1]), pk(hv[2], hv[3])} : u32x2{0u, 0u};
                }
                bar();
            }
            f32x4 d[4];
#pragma unroll
            for (int u = 0; u < 4; ++u) d[u] = mfma(PA[s], trfrag(lds + vo[u] + s * 16384, 2048), f32x4{0.f, 0.f, 0.f, 0.f});
            v0 = fmaf(f0[s], sel4(g, d[0][0], d[1][0], d[2][0], d[3][0]), v0);
            v1 = fmaf(f1[s], sel4(g, d[0][1], d[1][1], d[2][1], d[3][1]), v1);
        }
        st8(rws, u32x2{pk(v0, v1), tag}, opo, hblk + WS_OP + j * 2048);
        SB;
    }
    tb = FLAG(F_TE);
    u32x4 og[4];
    f32x2 wp[4];
#define OLOAD _Pragma("unroll") for (int i = 0; i < 4; ++i) og[i] = ld16<AUX_SC1>(rws, (l >> 3) * 2048 + (l & 7) * 16, hblk + WS_OP + i * 16384 + (j >> 1) * 128)
    u32 spo, bo, ao[4], la = L_A + ((l >> 5) * 8 + (l & 7)) * 4;
    {
        const u32 cc = c * 16 + (l >> 2);
        spo = cc < 56 ? (cc & 7) * WSBLK + (cc >> 3) * SP_STRIDE + fc * 64 + (l & 3) * 16 : OOR;
        bo = L_W2 + (((8 * g) & (odd ? 15 : 31)) + ((l >> 2) & 3)) * 896 + c * 256 + (l & 3) * 8;
#pragma unroll
        for (int u = 0; u < 4; ++u) { ao[u] = ((l >> 2) & 3) == u ? L_A + g * 16 : L_ZERO; PIN(ao[u]); }
        PIN(spo); PIN(bo); PIN(la);
    }
    if (cw0) {
        UNTIL(SX);
        SLOAD; SB;
        { const u32 tw = now() + HWAIT; while ((int)(now() - tw) < 0) ; }
        UNTIL(HX);
        OLOAD; SB;
        POLL(none(bad4(sg[0], tag) | bad4(sg[1], tag) | bad4(sg[2], tag) | bad4(sg[3], tag)), SLOAD)
#pragma unroll
        for (int hd = 0; hd < 2; ++hd) {
            const float M = max_hi3(fmx(fmx(v24(sg[0][2 * hd]), v24(sg[1][2 * hd])), fmx(v24(sg[2][2 * hd]), v24(sg[3][2 * hd]))));
            float L = 0.f;
#pragma unroll
            for (int i = 0; i < 4; ++i) { wp[i][hd] = __builtin_amdgcn_exp2f((v24(sg[i][2 * hd]) - M) * SCL); L = fmaf(wp[i][hd], v24(sg[i][2 * hd + 1]), L); }
            const float rl = __builtin_amdgcn_rcpf(sum_hi3(L));
#pragma unroll
            for (int i = 0; i < 4; ++i) wp[i][hd] *= rl;
        }
        PIN(wp[0]); PIN(wp[1]); PIN(wp[2]); PIN(wp[3]); SB;
    }
    LWAIT(FLAG(F_PROG) == 156);
    u32x4 B2[8];
#pragma unroll
    for (int u = 0; u < 8; ++u) B2[u] = trfrag(lds + bo - (c == 3 ? ((u + 2) >> 2) * 64 : 0) + u * 32, 4 * 896);
    if (!odd) {
        if (c == 0) {
            POLL(none(bad(og[0], tag) | bad(og[1], tag) | bad(og[2], tag) | bad(og[3], tag)), OLOAD)
            f32x2 a0 = {0.f, 0.f}, a1 = a0;
#pragma unroll
            for (int i = 0; i < 4; ++i) {
                a0 = __builtin_elementwise_fma(wp[i], f32x2{blo(og[i][0]), bhi(og[i][0])}, a0);
                a1 = __builtin_elementwise_fma(wp[i], f32x2{blo(og[i][2]), bhi(og[i][2])}, a1);
            }
            float o0 = tr32(a0[0], a0[1]), o1 = tr32(a1[0], a1[1]);
            o0 = tr16(o0, o0); o1 = tr16(o1, o1);
            o0 += dpp<ROR8>(o0); o1 += dpp<ROR8>(o1);
            LD(u32, la) = pk(o0, o1);
        }
        bar();
    }
    {
        f32x4 da = {0.f, 0.f, 0.f, 0.f}, db = da;
        if (!odd || ffn) {
            u32x4 A2[4];
#pragma unroll
            for (int u = 0; u < 4; ++u) A2[u] = LD(u32x4, ao[u]);
#pragma unroll
            for (int u = 0; u < 4; ++u) { da = mfma(A2[u], B2[u], da); db = mfma(A2[u], B2[u + 4], db); }
        }
        st16(rws, u32x4{asu(da[0]), tag, asu(db[0]), tag}, spo, odd ? WS_FP : WS_SP);
        SB;
    }
    if (odd && fc < 56) {
        const u32 o0 = (fc & 7) * WSBLK + (fc >> 3) * SP_STRIDE + c * 2048 + l * 16;
        u32 lf = L_FIN + ((l & 3) * 2 + (l >> 5)) * 16 + c * 4;
        PIN(lf);
        u32x4 g0, g1, g2, g3;
        float gate;
#define FLOAD g2 = ld16<AUX_SC1>(rws, o0, WS_FP); g3 = ld16<AUX_SC1>(rws, o0, WS_FP + 1024)
#define PLOAD g0 = ld16<AUX_SC1>(rws, o0, WS_SP); g1 = ld16<AUX_SC1>(rws, o0, WS_SP + 1024)
        UNTIL(GX);
        FLOAD; SB;
        {
            POLL(none(bad(g2, tag) | bad(g3, tag)), FLOAD)
            float f = tr32(asf(g2[0]) + asf(g3[0]), asf(g2[2]) + asf(g3[2]));
            f = tr16(f, f); f += dpp<ROR8>(f); f += dpp<ROR4>(f);
            LD(float, lf + 128) = f;
            bar();
            const f32x4 r = LD(f32x4, L_FIN + 128 + (l & 7) * 16);
            gate = rbf(silu(rbf(r[0] + r[1] + r[2] + r[3])));
            PIN(gate);
        }
        __builtin_amdgcn_s_sleep(FD);
        PLOAD;
        POLL(none(bad(g0, tag) | bad(g1, tag)), PLOAD)
        float s = tr32(asf(g0[0]) + asf(g1[0]), asf(g0[2]) + asf(g1[2]));
        s = tr16(s, s); s += dpp<ROR8>(s); s += dpp<ROR4>(s);
        LD(float, lf) = s;
        bar();
        if (c == 0) {
            const f32x4 r = LD(f32x4, L_FIN + (l & 7) * 16);
            const float y = blo(xo) + rbf(rbf(r[0] + r[1] + r[2] + r[3]) * gate);
            __builtin_amdgcn_raw_buffer_store_b16(__builtin_bit_cast(u16, (__bf16)y), mk(a_out, D * 2), l < 8 ? ocol * 2 : OOR, 0, 0);
            if (fc == 0) __builtin_amdgcn_raw_buffer_store_b32(tag, rws, l == 0 ? 0u : OOR, 0, AUX_SC1);
        }
    }
}

extern "C" void kernel_launch(void* const* d_in, const int* in_sizes, int n_in, void* d_out, int out_size, void* d_ws, size_t ws_size, hipStream_t stream) {
    fused_kernel<<<256, 320, 0, stream>>>(d_in[0], d_ws, d_in[1], d_in[2], d_out, d_in[4], d_in[8], d_in[3], d_in[6], d_in[7]);
}
```
